```python
import torch

def _softmax(x, axis=-1):
    return torch.softmax(x, dim=axis)
def _gelu(x):
    return torch.nn.functional.gelu(x, approximate="tanh")
def _silu(x):
    return torch.nn.functional.silu(x)
def _sigmoid(x):
    return torch.sigmoid(x)
def _layernorm(x, axis=-1, eps=1e-5):
    m = x.mean(dim=axis, keepdim=True)
    v = x.var(dim=axis, keepdim=True, unbiased=False)
    return (x - m) / torch.sqrt(v + eps)
def _scatter_add(tgt, idx, src, axis=0):
    return tgt.clone().index_add_(axis, idx, src)
def _segment_sum(src, seg, n):
    out = torch.zeros((n,) + src.shape[1:], dtype=src.dtype)
    return out.index_add_(0, seg, src)
def _pad(x, pw):
    flat = tuple(v for pair in reversed(pw) for v in pair)
    return torch.nn.functional.pad(x, flat)

kernel_name = 'recurrent_scan__spectral_gfnet__glu_gated_reglu__prompt'

def setup_inputs():

    _w = (lambda a: (lambda t: t.to(torch.bfloat16) if t.is_floating_point() else t)(torch.as_tensor(a)))
    def _A_setup():
        B, S, D = 1, 2048, 512
        Df = D // 2 + 1
        return {
            "x": _w((lambda *s: torch.randn(*s, dtype=torch.bfloat16) * 0.2)(B, S, D)),
            "w_r": _w((lambda *s: torch.randn(*s, dtype=torch.bfloat16) * 1.6)(S, Df) * 0.02),
            "w_i": _w((lambda *s: torch.randn(*s, dtype=torch.bfloat16) * 1.6)(S, Df) * 0.02),
        }
    _A_in = _A_setup()

    def _B_setup():
        B, S, D, F = 1, 2048, 512, 1792
        return {
            "x": _w((lambda *s: torch.randn(*s, dtype=torch.bfloat16) * 0.2)(B, S, D)),
            "W_gate": _w((lambda *s: torch.randn(*s, dtype=torch.bfloat16) * 0.8)(D, F) * (2.0 / D) ** 0.5),
            "W_up": _w((lambda *s: torch.randn(*s, dtype=torch.bfloat16) * 0.8)(D, F) * (2.0 / D) ** 0.5),
            "W_down": _w((lambda *s: torch.randn(*s, dtype=torch.bfloat16) * 0.8)(F, D) * (1.0 / F) ** 0.5),
        }
    _B_in = _B_setup()

    return {
        "A_x": _A_in["x"],
        "A_w_r": _A_in["w_r"],
        "A_w_i": _A_in["w_i"],
        "B_x": _B_in["x"],
        "B_W_gate": _B_in["W_gate"],
        "B_W_up": _B_in["W_up"],
        "B_W_down": _B_in["W_down"],
    }

def reference(A_x, A_w_r, A_w_i, B_x, B_W_gate, B_W_up, B_W_down):
    def _A_ref(x, w_r, w_i):
        B, S, D = x.shape
        X = (lambda x, axes=(-2,-1): torch.fft.rfft2(x.float(), dim=axes))(x, axes=(-2, -1))
        W = w_r + 1j * w_i
        X = X * W
        y = (lambda x, s, axes=(-2,-1): torch.fft.irfft2(x, s=s, dim=axes).to(torch.bfloat16))(X, (S, D), axes=(-2, -1))
        return y

    def _B_ref(x, W_gate, W_up, W_down):
        gate = torch.einsum("bsd,df->bsf", x, W_gate)
        up = torch.einsum("bsd,df->bsf", x, W_up)
        gate = gate * (lambda x, dt: x.to(dt))(gate > 0, gate.dtype)
        h = gate * up
        return torch.einsum("bsf,fd->bsd", h, W_down)

    x = A_x
    xn = _layernorm(x, axis=-1)
    s = _A_ref(xn, A_w_r, A_w_i)
    g = _silu(_B_ref(xn, B_W_gate, B_W_up, B_W_down))
    return x + s * g
```

```cpp
#include <hip/hip_runtime.h>
#include <stdint.h>
#pragma clang diagnostic ignored "-Wint-to-pointer-cast"

#define DI __device__ __forceinline__
#define LAS __attribute__((address_space(3)))
typedef unsigned short u16;
typedef __attribute__((ext_vector_type(8))) __bf16 bf16x8;
typedef __attribute__((ext_vector_type(4))) __bf16 bf16x4;
typedef __attribute__((ext_vector_type(2))) __bf16 bf16x2;
typedef __attribute__((ext_vector_type(8))) _Float16 f16x8;
typedef __attribute__((ext_vector_type(2))) _Float16 f16x2;
typedef __attribute__((ext_vector_type(2))) float f32x2;
typedef __attribute__((ext_vector_type(4))) float f32x4;
typedef __attribute__((ext_vector_type(16))) float f32x16;
typedef __attribute__((ext_vector_type(4))) unsigned u32x4;
typedef __attribute__((ext_vector_type(2))) unsigned u32x2;

namespace tb {
constexpr double PI = 3.14159265358979323846264338327950288;
constexpr double cos_small(double x) {
  double x2 = x * x, t = 1.0, s = 1.0;
  for (int i = 1; i <= 12; ++i) { t *= -x2 / ((2 * i - 1) * (2 * i)); s += t; }
  return s;
}
constexpr double sin_small(double x) {
  double x2 = x * x, t = x, s = x;
  for (int i = 1; i <= 12; ++i) { t *= -x2 / ((2 * i) * (2 * i + 1)); s += t; }
  return s;
}
constexpr double cos512(int i) {
  const int n = 512;
  i &= 511;
  if (i > n / 2) i = n - i;
  bool neg = false;
  if (i > n / 4) { i = n / 2 - i; neg = true; }
  double v = (i == n / 4) ? 0.0 : (i <= n / 8) ? cos_small(2 * PI * i / n) : sin_small(2 * PI * (n / 4 - i) / n);
  return neg ? -v : v;
}
constexpr u16 f2bf(double d) {
  float f = (float)d;
  unsigned u = __builtin_bit_cast(unsigned, f);
  u += 0x7fffu + ((u >> 16) & 1u);
  return (u16)(u >> 16);
}
constexpr u16 f2h(double d) {
  float f = (float)d;
  unsigned u = __builtin_bit_cast(unsigned, f);
  unsigned sign = (u >> 16) & 0x8000u;
  u &= 0x7fffffffu;
  if (u == 0) return (u16)sign;
  u += 0xfffu + ((u >> 13) & 1u);
  unsigned e = (u >> 23) - 112u, m = (u >> 13) & 0x3ffu;
  return (u16)(sign | (e << 10) | m);
}
struct Cos512 { u16 v[512]; };
constexpr Cos512 mk_cos_bf() { Cos512 c{}; for (int i = 0; i < 512; ++i) c.v[i] = f2bf(cos512(i)); return c; }
constexpr Cos512 mk_cos_h8() { Cos512 c{}; for (int i = 0; i < 512; ++i) c.v[i] = f2h(cos512(i) * 0.125); return c; }
constexpr Cos512 CB = mk_cos_bf();
constexpr Cos512 CH8 = mk_cos_h8();
struct Chunk { u16 v[16384]; };
constexpr Chunk mkF(int ci) {
  Chunk o{};
  const int which = ci >> 3, d0 = (ci & 7) * 64;
  for (int d = 0; d < 64; ++d) {
    const int dd = d0 + d;
    int idx = which ? 128 : 0;
    u16* p = o.v + d * 256;
    for (int c = 0; c < 256; ++c) { p[c] = CB.v[idx & 511]; idx += dd; }
    if (which) p[0] = CB.v[(dd & 1) * 256];
  }
  return o;
}
constexpr Chunk mkG(int dt) {
  Chunk o{};
  for (int ks = 0; ks < 32; ++ks)
    for (int l = 0; l < 64; ++l) {
      const int d = 32 * dt + (l & 31);
      const int k0 = 16 * ks + 8 * (l >> 5);
      const int which = k0 >> 8;
      int idx = (which ? 128 : 0) + (k0 & 255) * d;
      u16* p = o.v + (ks * 64 + l) * 8;
      for (int j = 0; j < 8; ++j) { p[j] = CB.v[idx & 511]; idx += d; }
      if (k0 == 256) p[0] = CB.v[(d & 1) * 256];
    }
  return o;
}
constexpr Chunk mkT64() {
  Chunk o{};
  for (int w = 0; w < 4; ++w)
    for (int ro = 0; ro < 2; ++ro)
      for (int s = 0; s < 4; ++s)
        for (int l = 0; l < 64; ++l)
          for (int e = 0; e < 8; ++e) {
            const int a = 16 * w + (l & 15), b = 16 * s + 4 * (l >> 4) + (e >> 1), ri = e & 1;
            const int th = 8 * a * b;
            int idx = th;
            if (ro == 0 && ri == 1) idx = th - 128;
            if (ro == 1 && ri == 0) idx = th + 128;
            o.v[((((w * 2 + ro) * 4 + s) * 64 + l) * 8) + e] = CH8.v[idx & 511];
          }
  return o;
}
struct Small { u16 v[2048]; };
constexpr Small mkT32() {
  Small o{};
  for (int cs = 0; cs < 2; ++cs)
    for (int u = 0; u < 2; ++u)
      for (int l = 0; l < 64; ++l)
        for (int j = 0; j < 8; ++j) {
          const int i = l & 15;
          const int a = 8 * (i >> 2) + 4 * u + (i & 3), b = 8 * (l >> 4) + j;
          const int th = 16 * a * b;
          o.v[(((cs * 2 + u) * 64 + l) * 8) + j] = CH8.v[(th - (cs ? 128 : 0)) & 511];
        }
  return o;
}
template <int I> struct FC { static constexpr Chunk v = mkF(I); };
template <int I> struct GC { static constexpr Chunk v = mkG(I); };
struct Tables { Chunk F[16]; Chunk G[16]; Chunk T64; Small T32; };
}
#define F_(i) tb::FC<i>::v
#define G_(i) tb::GC<i>::v
#if defined(__HIP_DEVICE_COMPILE__)
static __device__ __attribute__((aligned(256))) const tb::Tables g_tab = {
    {F_(0), F_(1), F_(2), F_(3), F_(4), F_(5), F_(6), F_(7), F_(8), F_(9), F_(10), F_(11), F_(12), F_(13), F_(14), F_(15)},
    {G_(0), G_(1), G_(2), G_(3), G_(4), G_(5), G_(6), G_(7), G_(8), G_(9), G_(10), G_(11), G_(12), G_(13), G_(14), G_(15)},
    tb::mkT64(), tb::mkT32()};
#else
static __device__ __attribute__((aligned(256))) const tb::Tables g_tab = {};
#endif

constexpr size_t WS_H = 0;
constexpr size_t WS_X1 = WS_H + 64ull * 112 * 1024;
constexpr size_t WS_WT = WS_X1 + 256ull * 8192;
constexpr size_t WS_Y1 = WS_WT + 260ull * 8192;
constexpr size_t WS_END = WS_Y1 + 512ull * 4096;

constexpr size_t WS_DBG = 64ull << 20;
#define STAMP(k, i)
#define STAMP_DECL
#define STAMP_OUT(k)
struct Params {
  const char* x; const char* wr; const char* wi; const char* wg; const char* wu; const char* wd;
  char* out; char* ws;
};

DI void glds16(unsigned voff, const void* sbase, unsigned ldsdst) {
  asm volatile("s_mov_b32 m0, %2\n\ts_nop 0\n\tglobal_load_lds_dwordx4 %0, %1" ::"v"(voff), "s"(sbase), "s"(ldsdst) : "memory");
}
template <int N> DI void wait_vm() { asm volatile("s_waitcnt vmcnt(%0)" ::"i"(N) : "memory"); }
template <int N> DI void wait_lgkm() { asm volatile("s_waitcnt lgkmcnt(%0)" ::"i"(N) : "memory"); }
DI void bar() { __builtin_amdgcn_s_barrier(); }
DI bf16x8 lds_rd128(unsigned a) { return *(const LAS bf16x8*)a; }
DI bf16x8 lds_tr2(unsigned a0, unsigned a1) {
  bf16x4 lo = __builtin_amdgcn_ds_read_tr16_b64_v4bf16((LAS bf16x4*)a0);
  bf16x4 hi = __builtin_amdgcn_ds_read_tr16_b64_v4bf16((LAS bf16x4*)a1);
  return __builtin_shufflevector(lo, hi, 0, 1, 2, 3, 4, 5, 6, 7);
}
DI float bfr(float v) { return (float)(__bf16)v; }
DI unsigned pk_bf(float a, float b) { f32x2 v = {a, b}; return __builtin_bit_cast(unsigned, __builtin_convertvector(v, bf16x2)); }
DI unsigned pk_h(float a, float b) { f32x2 v = {a, b}; return __builtin_bit_cast(unsigned, __builtin_convertvector(v, f16x2)); }
DI float bf_lo(unsigned u) { return __builtin_bit_cast(float, u << 16); }
DI float bf_hi(unsigned u) { return __builtin_bit_cast(float, u & 0xffff0000u); }
DI float dot2(unsigned a, unsigned b, float c) {
  return __builtin_amdgcn_fdot2_f32_bf16(__builtin_bit_cast(bf16x2, a), __builtin_bit_cast(bf16x2, b), c, false);
}
#define MFMA32(a, b, c) __builtin_amdgcn_mfma_f32_32x32x16_bf16((a), (b), (c), 0, 0, 0)
#define MFMA16H(a, b, c) __builtin_amdgcn_mfma_f32_16x16x32_f16((a), (b), (c), 0, 0, 0)

constexpr unsigned P1_X = 0, P1_W = 49152, P1_ST = 147456, P1_CS = P1_ST + 1024;

DI void phase1(const Params& P, int b) {
  STAMP_DECL STAMP(0, 0);
  const int tid = threadIdx.x, l = tid & 63;
  const int w = __builtin_amdgcn_readfirstlane(tid >> 6);
  const int xcd = b & 7, idx = b >> 3;
  const int p = (xcd & 1) * 8 + (idx & 7), j = (xcd >> 1) * 4 + (idx >> 3);
  const bool dft = j >= 14;
  const char* tabF = (const char*)&g_tab.F[0];
  const char* m1 = dft ? tabF + (j - 14) * 256 : P.wg + j * 256;
  const char* m2 = dft ? tabF + 262144 + (j - 14) * 256 : P.wu + j * 256;
  const unsigned ld = dft ? 512u : 3584u;
  const char* xs = P.x + (size_t)(p * 128 + w * 32) * 1024;
  m1 += (size_t)(8 * w) * ld; m2 += (size_t)(8 * w) * ld;
  const unsigned xo0 = (l >> 3) * 1024 + (((l & 7) ^ (l >> 4)) << 4);
  const unsigned xo1 = (l >> 3) * 1024 + (((l & 7) ^ (4 + (l >> 4))) << 4);
  const unsigned wo0 = (l >> 4) * ld + (((l & 15) ^ ((l >> 4) << 2)) << 4);
  const unsigned wo1 = (l >> 4) * ld + (((l & 15) ^ (((l >> 4) << 2) | 2)) << 4);

  auto issue_x = [&](int v) {
    const unsigned dst = P1_X + (v % 3) * 16384 + w * 4096;
    const char* s = xs + v * 128;
    glds16(xo0, s, dst);
    glds16(xo1, s + 8192, dst + 1024);
    glds16(xo0, s + 16384, dst + 2048);
    glds16(xo1, s + 24576, dst + 3072);
  };
  auto issue_w = [&](int u) {
    const unsigned dst = P1_W + (u % 6) * 16384 + w * 2048;
    const size_t o = (size_t)(32 * u) * ld;
    glds16(wo0, m1 + o, dst);
    glds16(wo0, m1 + o + 4 * ld, dst + 1024);
    glds16(wo1, m2 + o, dst + 8192);
    glds16(wo1, m2 + o + 4 * ld, dst + 8192 + 1024);
  };
  issue_w(0); issue_x(0); issue_w(1); issue_w(2); issue_x(1); issue_w(3); issue_w(4); issue_x(2); issue_w(5);

  const int h = l >> 5;
  unsigned xa[4];
#pragma unroll
  for (int jj = 0; jj < 4; ++jj) xa[jj] = P1_X + (((jj + w) & 3) * 32 + (l & 31)) * 128;
  const unsigned xf = (l >> 1) & 7;
  const int mm = (l >> 4) & 1, q = (l & 15) >> 2, pp = l & 3;
  unsigned wa[2];
#pragma unroll
  for (int tt = 0; tt < 2; ++tt)
    wa[tt] = P1_W + mm * 8192 + (8 * h + q) * 256 + (((4 * w + 2 * tt + (pp >> 1)) ^ ((q << 2) | (mm << 1))) << 4) + (pp & 1) * 8;

  f32x16 acc[4][2];
#pragma unroll
  for (int a = 0; a < 4; ++a)
#pragma unroll
    for (int t = 0; t < 2; ++t)
#pragma unroll
      for (int r = 0; r < 16; ++r) acc[a][t][r] = 0.f;
  float sx = 0.f, sq = 0.f, cs0 = 0.f, cs1 = 0.f;
  const unsigned ONES = 0x3f803f80u;

  bf16x8 xfA[4], wfA[2], xfB[4], wfB[2];
  auto load_frags = [&](int u, int ks, bf16x8 (&xfr)[4], bf16x8 (&wfr)[2]) {
    const unsigned ws = (u % 6) * 16384 + ks * 4096;
    const unsigned xsl = ((u >> 1) % 3) * 16384;
    const int ks4 = (u & 1) * 2 + ks;
#pragma unroll
    for (int tt = 0; tt < 2; ++tt) wfr[tt] = lds_tr2(wa[tt] + ws, wa[tt] + ws + 1024);
#pragma unroll
    for (int jj = 0; jj < 4; ++jj) xfr[jj] = lds_rd128(xa[jj] + xsl + (((2 * ks4 + h) ^ xf) << 4));
  };
  auto compute = [&](bf16x8 (&xfr)[4], bf16x8 (&wfr)[2]) {
#pragma unroll
    for (int jj = 0; jj < 4; ++jj)
#pragma unroll
      for (int tt = 0; tt < 2; ++tt) acc[jj][tt] = MFMA32(wfr[tt], xfr[jj], acc[jj][tt]);
    const u32x4 xu = __builtin_bit_cast(u32x4, xfr[0]);
    const u32x4 w0 = __builtin_bit_cast(u32x4, wfr[0]), w1 = __builtin_bit_cast(u32x4, wfr[1]);
#pragma unroll
    for (int i = 0; i < 4; ++i) {
      sx = dot2(xu[i], ONES, sx); sq = dot2(xu[i], xu[i], sq);
      cs0 = dot2(w0[i], ONES, cs0); cs1 = dot2(w1[i], ONES, cs1);
    }
    asm volatile("" : "+v"(sx), "+v"(sq), "+v"(cs0), "+v"(cs1));
  };

  STAMP(0, 1);
  wait_vm<28>(); bar();
  STAMP(0, 2);
  load_frags(0, 0, xfA, wfA);
#define P1_STEP(u, NV)                                                     \
  {                                                                        \
    load_frags(u, 1, xfB, wfB);                                            \
    compute(xfA, wfA);                                                     \
    __builtin_amdgcn_sched_barrier(0);                                     \
    if ((u) < 15) {                                                        \
      wait_vm<NV>(); wait_lgkm<0>(); bar();                                \
      if ((u) + 6 < 16) issue_w_x(u);                                      \
      load_frags((u) + 1, 0, xfA, wfA);                                    \
    }                                                                      \
    compute(xfB, wfB);                                                     \
    __builtin_amdgcn_sched_barrier(0);                                     \
  }
  auto issue_w_x = [&](int u) {
    if (u & 1) { issue_x((u >> 1) + 3); issue_w(u + 6); }
    else issue_w(u + 6);
  };
  P1_STEP(0, 4 * (9 - 3));
  P1_STEP(1, 4 * (10 - 5));
  P1_STEP(2, 4 * (12 - 6));
  P1_STEP(3, 4 * (13 - 8));
  P1_STEP(4, 4 * (15 - 9));
  P1_STEP(5, 4 * (16 - 11));
  P1_STEP(6, 4 * (18 - 12));
  P1_STEP(7, 4 * (19 - 14));
  STAMP(0, 3);
  P1_STEP(8, 4 * (21 - 15));
  P1_STEP(9, 4 * (22 - 17));
  P1_STEP(10, 4 * (24 - 18));
  P1_STEP(11, 4 * (24 - 20));
  P1_STEP(12, 4 * (24 - 21));
  P1_STEP(13, 4 * (24 - 23));
  P1_STEP(14, 0);
  unsigned wt[8];
  unsigned wt256[8];
  {
    const int c = 16 * j + (tid & 15), k1 = 16 * (p >> 2) + (tid >> 4);
    const u16* wr = (const u16*)P.wr; const u16* wi = (const u16*)P.wi;
#pragma unroll
    for (int e = 0; e < 8; ++e) {
      const int k = k1 + 64 * (8 * (p & 3) + e);
      wt[e] = (unsigned)wr[k * 257 + c] | ((unsigned)wi[k * 257 + c] << 16);
    }
    if (j == 0 && (tid & 15) == 0) {
#pragma unroll
      for (int e = 0; e < 8; ++e) {
        const int k = k1 + 64 * (8 * (p & 3) + e);
        wt256[e] = (unsigned)wr[k * 257 + 256] | ((unsigned)wi[k * 257 + 256] << 16);
      }
    }
  }
  P1_STEP(15, 0);
#undef P1_STEP

  STAMP(0, 4);
  sx += __shfl_xor(sx, 32); sq += __shfl_xor(sq, 32);
  cs0 += __shfl_xor(cs0, 32); cs1 += __shfl_xor(cs1, 32);
  {
    const float mean = sx * (1.f / 512.f);
    const float var = fmaxf(sq * (1.f / 512.f) - mean * mean, 0.f);
    const float mb = bfr(mean);
    const float sd = bfr(sqrtf(bfr(bfr(var) + 1e-5f)));
    f32x2 st = {mb, 1.f / sd};
    if (l < 32) {
      *(LAS f32x2*)(P1_ST + (w * 32 + l) * 8) = st;
      *(LAS float*)(P1_CS + (w * 64 + l) * 4) = cs0;
      *(LAS float*)(P1_CS + (w * 64 + 32 + l) * 4) = cs1;
    }
  }
  wait_lgkm<0>(); bar();
  f32x2 st[4];
#pragma unroll
  for (int jj = 0; jj < 4; ++jj) st[jj] = *(const LAS f32x2*)(P1_ST + (((jj + w) & 3) * 32 + (l & 31)) * 8);
  f32x4 cs[2][4];
#pragma unroll
  for (int tt = 0; tt < 2; ++tt)
#pragma unroll
    for (int g = 0; g < 4; ++g) cs[tt][g] = *(const LAS f32x4*)(P1_CS + (w * 64 + tt * 32 + 8 * g + 4 * h) * 4);
#pragma unroll
  for (int jj = 0; jj < 4; ++jj)
#pragma unroll
    for (int tt = 0; tt < 2; ++tt)
#pragma unroll
      for (int r = 0; r < 16; ++r) acc[jj][tt][r] = (acc[jj][tt][r] - st[jj][0] * cs[tt][r >> 2][r & 3]) * st[jj][1];

  if (!dft) {
#pragma unroll
    for (int jj = 0; jj < 4; ++jj)
#pragma unroll
      for (int tt = 0; tt < 2; ++tt) {
        u32x4 o;
#pragma unroll
        for (int i = 0; i < 4; ++i) {
          const unsigned g = pk_bf(acc[jj][tt][2 * i], acc[jj][tt][2 * i + 1]);
          const unsigned u = pk_bf(acc[jj][tt][8 + 2 * i], acc[jj][tt][9 + 2 * i]);
          o[i] = pk_bf(fmaxf(bf_lo(g), 0.f) * bf_lo(u), fmaxf(bf_hi(g), 0.f) * bf_hi(u));
        }
        const int rt = p * 4 + ((jj + w) & 3), ks = j * 8 + 2 * w + tt;
        *(u32x4*)(P.ws + WS_H + ((size_t)(rt * 112 + ks) * 64 + l) * 16) = o;
      }
  } else {
    const int n2 = l & 31;
    const unsigned lo = ((n2 >> 2) & 1) * 4096 + (p >> 2) * 1024 + ((p & 3) * 16 + 4 * (n2 >> 3) + (n2 & 3)) * 16;
#pragma unroll
    for (int tt = 0; tt < 2; ++tt)
#pragma unroll
      for (int r = 0; r < 8; ++r) {
        const int c = (j - 14) * 128 + (2 * w + tt) * 16 + (r & 3) + 8 * (r >> 2) + 4 * h;
        unsigned v[4];
#pragma unroll
        for (int jj = 0; jj < 4; ++jj) v[jj] = pk_h(acc[jj][tt][r], acc[jj][tt][r + 8]);
        u32x4 o;
        if (w == 0) o = u32x4{v[0], v[1], v[2], v[3]};
        else if (w == 1) o = u32x4{v[3], v[0], v[1], v[2]};
        else if (w == 2) o = u32x4{v[2], v[3], v[0], v[1]};
        else o = u32x4{v[1], v[2], v[3], v[0]};
        *(u32x4*)(P.ws + WS_X1 + (size_t)c * 8192 + lo) = o;
      }
  }
  STAMP(0, 5);
  {
    const int c = 16 * j + (tid & 15);
    char* d = P.ws + WS_WT + (size_t)c * 8192 + (p >> 2) * 2048 + ((p & 3) * 16 + (tid >> 4)) * 32;
    *(u32x4*)d = u32x4{wt[0], wt[1], wt[2], wt[3]};
    *(u32x4*)(d + 16) = u32x4{wt[4], wt[5], wt[6], wt[7]};
    if (j == 0 && (tid & 15) == 0) {
      d = P.ws + WS_WT + (size_t)256 * 8192 + (p >> 2) * 2048 + ((p & 3) * 16 + (tid >> 4)) * 32;
      *(u32x4*)d = u32x4{wt256[0], wt256[1], wt256[2], wt256[3]};
      *(u32x4*)(d + 16) = u32x4{wt256[4], wt256[5], wt256[6], wt256[7]};
    }
  }
  STAMP(0, 6);
  STAMP(0, 7);
  STAMP_OUT(0);
}

DI f16x8 pack8h(const f32x4& a, const f32x4& b) {
  u32x4 o = {pk_h(a[0], a[1]), pk_h(a[2], a[3]), pk_h(b[0], b[1]), pk_h(b[2], b[3])};
  return __builtin_bit_cast(f16x8, o);
}
DI f16x8 negh(const f16x8& a) {
  u32x4 o = __builtin_bit_cast(u32x4, a);
  o ^= 0x80008000u;
  return __builtin_bit_cast(f16x8, o);
}
constexpr unsigned PB_EX = 0, PB_MX = 8192;

DI void phaseB(const Params& P, int col) {
  STAMP_DECL STAMP(1, 0);
  const int tid = threadIdx.x, l = tid & 63;
  const int w = __builtin_amdgcn_readfirstlane(tid >> 6);
  const int q = l >> 4;
  const char* t64 = (const char*)&g_tab.T64 + w * 8192 + l * 16;
  const char* t32 = (const char*)&g_tab.T32 + l * 16;
  f16x8 c64[2][4], c32[2][2], z[2][4];
  const char* xp = P.ws + WS_X1 + (size_t)(col & 255) * 8192 + l * 16;
#pragma unroll
  for (int t = 0; t < 2; ++t)
#pragma unroll
    for (int s = 0; s < 4; ++s) z[t][s] = *(const f16x8*)(xp + t * 4096 + s * 1024);
  const char* wp = P.ws + WS_WT + (size_t)col * 8192 + w * 2048 + l * 32;
  const u32x4 wv0 = *(const u32x4*)wp, wv1 = *(const u32x4*)(wp + 16);
#pragma unroll
  for (int ro = 0; ro < 2; ++ro)
#pragma unroll
    for (int s = 0; s < 4; ++s) c64[ro][s] = *(const f16x8*)(t64 + (ro * 4 + s) * 1024);
#pragma unroll
  for (int cs = 0; cs < 2; ++cs)
#pragma unroll
    for (int u = 0; u < 2; ++u) c32[cs][u] = *(const f16x8*)(t32 + (cs * 2 + u) * 1024);
  float twc[8], tws[8];
  const int k1 = 16 * w + (l & 15);
#pragma unroll
  for (int jx = 0; jx < 8; ++jx) {
    const float a = (float)((8 * q + jx) * k1) * (1.f / 2048.f);
    twc[jx] = __builtin_amdgcn_cosf(a); tws[jx] = __builtin_amdgcn_sinf(a);
  }
  if ((col & 255) == 0) {
#pragma unroll
    for (int t = 0; t < 2; ++t)
#pragma unroll
      for (int s = 0; s < 4; ++s) {
        u32x4 v = __builtin_bit_cast(u32x4, z[t][s]);
        if (col == 0) v &= 0xffffu; else v >>= 16;
        z[t][s] = __builtin_bit_cast(f16x8, v);
      }
  }
  float wre[8], wim[8];
  unsigned mx = 0;
#pragma unroll
  for (int e = 0; e < 8; ++e) {
    const unsigned v = e < 4 ? wv0[e] : wv1[e - 4];
    wre[e] = bf_lo(v); wim[e] = bf_hi(v);
    mx = max(mx, max(v & 0x7fff0000u, (v << 16) & 0x7fff0000u));
  }
#pragma unroll
  for (int o = 32; o > 0; o >>= 1) mx = max(mx, (unsigned)__shfl_xor((int)mx, o));
  if (l == 0) *(LAS unsigned*)(PB_MX + w * 4) = mx;

  STAMP(1, 1);
  STAMP(1, 2);
  const f32x4 zero4 = {0.f, 0.f, 0.f, 0.f};
  f32x4 ar[2], ai[2];
#pragma unroll
  for (int t = 0; t < 2; ++t) {
    ar[t] = zero4; ai[t] = zero4;
#pragma unroll
    for (int s = 0; s < 4; ++s) { ar[t] = MFMA16H(z[t][s], c64[0][s], ar[t]); ai[t] = MFMA16H(z[t][s], c64[1][s], ai[t]); }
  }
#pragma unroll
  for (int t = 0; t < 2; ++t)
#pragma unroll
    for (int r = 0; r < 4; ++r) {
      const float c = twc[4 * t + r], s = tws[4 * t + r], xr = ar[t][r], xi = ai[t][r];
      ar[t][r] = xr * c + xi * s; ai[t][r] = xi * c - xr * s;
    }
  f16x8 fr = pack8h(ar[0], ar[1]), fi = pack8h(ai[0], ai[1]), fn = negh(fr);
#pragma unroll
  for (int u = 0; u < 2; ++u) {
    ar[u] = MFMA16H(c32[0][u], fr, zero4); ar[u] = MFMA16H(c32[1][u], fi, ar[u]);
    ai[u] = MFMA16H(c32[0][u], fi, zero4); ai[u] = MFMA16H(c32[1][u], fn, ai[u]);
  }
  wait_lgkm<0>(); bar();
  STAMP(1, 3);
  const u32x4 m4 = *(const LAS u32x4*)(PB_MX);
  unsigned ex = max(max(m4[0], m4[1]), max(m4[2], m4[3])) >> 23;
  ex = min(max(ex, 4u), 250u);
  const float wsc = __builtin_bit_cast(float, (252u - ex) << 23);
  const float osc0 = __builtin_bit_cast(float, (ex + 2u) << 23);
#pragma unroll
  for (int u = 0; u < 2; ++u)
#pragma unroll
    for (int r = 0; r < 4; ++r) {
      const float c = wre[4 * u + r] * wsc, s = wim[4 * u + r] * wsc, xr = ar[u][r], xi = ai[u][r];
      ar[u][r] = xr * c - xi * s; ai[u][r] = xi * c + xr * s;
    }
  fr = pack8h(ar[0], ar[1]); fi = pack8h(ai[0], ai[1]); fn = negh(fi);
#pragma unroll
  for (int t = 0; t < 2; ++t) {
    ar[t] = MFMA16H(c32[0][t], fr, zero4); ar[t] = MFMA16H(c32[1][t], fn, ar[t]);
    ai[t] = MFMA16H(c32[0][t], fi, zero4); ai[t] = MFMA16H(c32[1][t], fr, ai[t]);
  }
#pragma unroll
  for (int t = 0; t < 2; ++t)
#pragma unroll
    for (int r = 0; r < 4; ++r) {
      const float c = twc[4 * t + r], s = tws[4 * t + r], xr = ar[t][r], xi = ai[t][r];
      const unsigned v = pk_h(xr * c - xi * s, -(xi * c + xr * s));
      *(LAS unsigned*)(PB_EX + t * 4096 + w * 1024 + (((l >> 2) & 3) * 16 + 4 * q + r) * 16 + (l & 3) * 4) = v;
    }
  wait_lgkm<0>(); bar();
#pragma unroll
  for (int t = 0; t < 2; ++t)
#pragma unroll
    for (int s = 0; s < 4; ++s) z[t][s] = *(const LAS f16x8*)(PB_EX + t * 4096 + s * 1024 + l * 16);
#pragma unroll
  for (int t = 0; t < 2; ++t) {
    ar[t] = zero4; ai[t] = zero4;
#pragma unroll
    for (int s = 0; s < 4; ++s) { ar[t] = MFMA16H(z[t][s], c64[0][s], ar[t]); ai[t] = MFMA16H(z[t][s], c64[1][s], ai[t]); }
  }
  STAMP(1, 4);
  const float k0 = ((col & 255) == 0) ? (2.f / 512.f) : (4.f / 512.f);
  u32x4 ore, oim;
#pragma unroll
  for (int t = 0; t < 2; ++t)
#pragma unroll
    for (int i = 0; i < 2; ++i) {
      ore[2 * t + i] = pk_bf(ar[t][2 * i] * k0 * osc0, ar[t][2 * i + 1] * k0 * osc0);
      oim[2 * t + i] = pk_bf(-ai[t][2 * i] * k0 * osc0, -ai[t][2 * i + 1] * k0 * osc0);
    }
  char* yp = P.ws + WS_Y1 + ((16 * w + (l & 15)) * 32 + 8 * q) * 2;
  *(u32x4*)(yp + (size_t)col * 4096) = ore;
  if ((col & 255) != 0) *(u32x4*)(yp + (size_t)(256 + col) * 4096) = oim;
  STAMP(1, 5);
  STAMP(1, 6);
  STAMP_OUT(1);
}

DI void gload16(u32x4& dst, unsigned voff, const void* sbase) {
  asm volatile("global_load_dwordx4 %0, %1, %2" : "=v"(dst) : "v"(voff), "s"(sbase) : "memory");
}
template <int N> DI void wait_vm2(u32x4& a, u32x4& b) { asm volatile("s_waitcnt vmcnt(%2)" : "+v"(a), "+v"(b) : "i"(N) : "memory"); }
constexpr int GD = 15;

template <int I> struct GStep {};

DI void phaseG(const Params& P, int b) {
  STAMP_DECL STAMP(2, 0);
  const int tid = threadIdx.x, l = tid & 63;
  const int w = __builtin_amdgcn_readfirstlane(tid >> 6);
  const int xcd = b & 7, idx = b >> 3;
  const int r64 = (xcd >> 1) * 8 + (idx & 7), t64 = (xcd & 1) * 4 + (idx >> 3);
  const int wa_ = w >> 1, wb_ = w & 1;
  const unsigned ring = w * 32768;
  const int h = l >> 5;
  const char* hs0 = P.ws + WS_H + (size_t)((2 * r64 + wa_) * 112 + 28 * w) * 1024;
  const char* hs1 = P.ws + WS_H + (size_t)((2 * r64 + (wa_ ^ 1)) * 112 + 28 * w) * 1024;
  const char* wds = P.wd + (size_t)(448 * w) * 1024 + t64 * 128;
  const char* ys = P.ws + WS_Y1 + (size_t)(128 * w) * 4096 + r64 * 128;
  const char* gs0 = (const char*)&g_tab.G[0] + (size_t)((2 * t64 + wb_) * 32 + 8 * w) * 1024;
  const char* gs1 = (const char*)&g_tab.G[0] + (size_t)((2 * t64 + (wb_ ^ 1)) * 32 + 8 * w) * 1024;
  const unsigned swz = ((l & 7) ^ (((l >> 4) & 1) << 2)) << 4;
  const unsigned wdo = (l >> 3) * 1024 + swz, yo = (l >> 3) * 4096 + swz, lo16 = l * 16;
  const int orow = r64 * 64 + wa_ * 32 + (l & 31), ocol = t64 * 64 + wb_ * 32 + 4 * h;
  u32x2 xv[4];
#pragma unroll
  for (int g = 0; g < 4; ++g) xv[g] = *(const u32x2*)(P.x + (size_t)orow * 1024 + (ocol + 8 * g) * 2);
  wait_vm<0>();
  asm volatile("" : "+v"(xv[0]), "+v"(xv[1]), "+v"(xv[2]), "+v"(xv[3]));

  STAMP(2, 1);
  u32x4 rf[GD][2];
  auto issue = [&](int i) {
    const unsigned dst = ring + (i & 15) * 2048;
    if (i < 28) {
      glds16(wdo, wds + i * 16384, dst);
      glds16(wdo, wds + i * 16384 + 8192, dst + 1024);
      gload16(rf[i % GD][0], lo16, hs0 + i * 1024);
      gload16(rf[i % GD][1], lo16, hs1 + i * 1024);
    } else {
      glds16(yo, ys + (size_t)(i - 28) * 65536, dst);
      glds16(yo, ys + (size_t)(i - 28) * 65536 + 32768, dst + 1024);
      gload16(rf[i % GD][0], lo16, gs0 + (i - 28) * 1024);
      gload16(rf[i % GD][1], lo16, gs1 + (i - 28) * 1024);
    }
  };
  const int blk = (l >> 4) & 1, q = (l & 15) >> 2, pp = l & 3;
  unsigned ta[2], tc[2];
#pragma unroll
  for (int t = 0; t < 2; ++t) {
    ta[t] = ring + (4 * h + q) * 128 + (((4 * (t ^ wb_) + 2 * blk + (pp >> 1)) ^ (((q >> 1) & 1) << 2)) << 4) + (pp & 1) * 8;
    tc[t] = ring + (8 * h + q) * 128 + (((4 * (t ^ wa_) + 2 * blk + (pp >> 1)) ^ (((q >> 1) & 1) << 2)) << 4) + (pp & 1) * 8;
  }
  f32x16 ao[2][2], as[2][2];
#pragma unroll
  for (int a = 0; a < 2; ++a)
#pragma unroll
    for (int t = 0; t < 2; ++t)
#pragma unroll
      for (int r = 0; r < 16; ++r) { ao[a][t][r] = 0.f; as[a][t][r] = 0.f; }

#pragma unroll
  for (int i = 0; i < GD; ++i) issue(i);
  bf16x8 lf[2][2];
  auto rd = [&](int i) {
    const unsigned o = (i & 15) * 2048;
#pragma unroll
    for (int t = 0; t < 2; ++t)
      lf[i & 1][t] = i < 28 ? lds_tr2(ta[t] + o, ta[t] + o + 1024) : lds_tr2(tc[t] + o, tc[t] + o + 512);
  };
  STAMP(2, 2);
  wait_vm<4 * (GD - 1) + 2>();
  STAMP(2, 3);
  rd(0);
#define G_STEP(i)                                                                                         \
  {                                                                                                       \
               \
    constexpr int last = ((i) + GD - 1 < 35) ? (i) + GD - 1 : 35;                                          \
    constexpr int nv = ((i) < 35) ? 4 * (last - (i) - 1) + 2 : 0;                                          \
    wait_vm2<nv>(rf[(i) % GD][0], rf[(i) % GD][1]);                                                        \
    if ((i) < 35) rd((i) + 1);                                                                             \
    const bf16x8 r0 = __builtin_bit_cast(bf16x8, rf[(i) % GD][0]), r1 = __builtin_bit_cast(bf16x8, rf[(i) % GD][1]); \
    if ((i) < 28) {                                                                                        \
      ao[0][0] = MFMA32(lf[(i) & 1][0], r0, ao[0][0]); ao[0][1] = MFMA32(lf[(i) & 1][1], r0, ao[0][1]);     \
      ao[1][0] = MFMA32(lf[(i) & 1][0], r1, ao[1][0]); ao[1][1] = MFMA32(lf[(i) & 1][1], r1, ao[1][1]);     \
    } else {                                                                                               \
      as[0][0] = MFMA32(r0, lf[(i) & 1][0], as[0][0]); as[0][1] = MFMA32(r1, lf[(i) & 1][0], as[0][1]);     \
      as[1][0] = MFMA32(r0, lf[(i) & 1][1], as[1][0]); as[1][1] = MFMA32(r1, lf[(i) & 1][1], as[1][1]);     \
    }                                                                                                      \
    __builtin_amdgcn_sched_barrier(0);                                                                     \
    if ((i) + GD < 36) issue((i) + GD);                                                                    \
  }
  G_STEP(0) G_STEP(1) G_STEP(2) G_STEP(3) G_STEP(4) G_STEP(5) G_STEP(6) G_STEP(7) G_STEP(8)
  G_STEP(9) G_STEP(10) G_STEP(11) G_STEP(12) G_STEP(13) G_STEP(14) G_STEP(15) G_STEP(16) G_STEP(17)
  STAMP(2, 4);
  G_STEP(18) G_STEP(19) G_STEP(20) G_STEP(21) G_STEP(22) G_STEP(23) G_STEP(24) G_STEP(25) G_STEP(26)
  G_STEP(27) G_STEP(28) G_STEP(29) G_STEP(30) G_STEP(31) G_STEP(32) G_STEP(33) G_STEP(34) G_STEP(35)
#undef G_STEP
  STAMP(2, 5);
  wait_lgkm<0>(); bar();
#pragma unroll
  for (int jj = 1; jj < 4; ++jj) {
    const unsigned base = ((jj ^ w) * 3 + (jj - 1)) * 8192 + l * 16;
#pragma unroll
    for (int g = 0; g < 4; ++g) {
      const f32x16& o = ao[jj >> 1][jj & 1]; const f32x16& s = as[jj >> 1][jj & 1];
      *(LAS f32x4*)(base + g * 1024) = f32x4{o[4 * g], o[4 * g + 1], o[4 * g + 2], o[4 * g + 3]};
      *(LAS f32x4*)(base + 4096 + g * 1024) = f32x4{s[4 * g], s[4 * g + 1], s[4 * g + 2], s[4 * g + 3]};
    }
  }
  wait_lgkm<0>(); bar();
#pragma unroll
  for (int jj = 0; jj < 3; ++jj) {
    const unsigned base = (w * 3 + jj) * 8192 + l * 16;
#pragma unroll
    for (int g = 0; g < 4; ++g) {
      const f32x4 o = *(const LAS f32x4*)(base + g * 1024), s = *(const LAS f32x4*)(base + 4096 + g * 1024);
#pragma unroll
      for (int e = 0; e < 4; ++e) { ao[0][0][4 * g + e] += o[e]; as[0][0][4 * g + e] += s[e]; }
    }
  }
#pragma unroll
  for (int g = 0; g < 4; ++g) {
    float r[4];
#pragma unroll
    for (int e = 0; e < 4; ++e) {
      const float o = bfr(ao[0][0][4 * g + e]);
      const float gl = bfr(o / (1.f + __expf(-o)));
      const float sg = bfr(bfr(as[0][0][4 * g + e]) * gl);
      const float xx = (e & 1) ? bf_hi(xv[g][e >> 1]) : bf_lo(xv[g][e >> 1]);
      r[e] = xx + sg;
    }
    *(u32x2*)(P.out + (size_t)orow * 1024 + (ocol + 8 * g) * 2) = u32x2{pk_bf(r[0], r[1]), pk_bf(r[2], r[3])};
  }
  STAMP(2, 6);
  STAMP(2, 7);
  STAMP_OUT(2);
}

__global__ __launch_bounds__(256, 1) void k_p1(Params P) { phase1(P, blockIdx.x); }
__global__ __launch_bounds__(256, 1) void k_pb(Params P) { phaseB(P, blockIdx.x); }
__global__ __launch_bounds__(256, 1) void k_pg(Params P) { phaseG(P, blockIdx.x); }

extern "C" void kernel_launch(void* const* d_in, const int* in_sizes, int n_in, void* d_out, int out_size,
                              void* d_ws, size_t ws_size, hipStream_t stream) {
  Params P;
  P.x = (const char*)d_in[0]; P.wr = (const char*)d_in[1]; P.wi = (const char*)d_in[2];
  P.wg = (const char*)d_in[4]; P.wu = (const char*)d_in[5]; P.wd = (const char*)d_in[6];
  P.out = (char*)d_out; P.ws = (char*)d_ws;
  (void)hipFuncSetAttribute((const void*)k_p1, hipFuncAttributeMaxDynamicSharedMemorySize, 160 * 1024);
  (void)hipFuncSetAttribute((const void*)k_pg, hipFuncAttributeMaxDynamicSharedMemorySize, 160 * 1024);
  k_p1<<<256, 256, 150 * 1024, stream>>>(P);
  k_pb<<<257, 256, 16 * 1024, stream>>>(P);
  k_pg<<<256, 256, 128 * 1024, stream>>>(P);
}
```

```cpp
#include <hip/hip_runtime.h>
#include <stdint.h>
#pragma clang diagnostic ignored "-Wint-to-pointer-cast"

#define DI __device__ __forceinline__
#define LAS __attribute__((address_space(3)))
typedef unsigned short u16;
typedef __attribute__((ext_vector_type(8))) __bf16 bf16x8;
typedef __attribute__((ext_vector_type(4))) __bf16 bf16x4;
typedef __attribute__((ext_vector_type(2))) __bf16 bf16x2;
typedef __attribute__((ext_vector_type(8))) _Float16 f16x8;
typedef __attribute__((ext_vector_type(2))) _Float16 f16x2;
typedef __attribute__((ext_vector_type(2))) float f32x2;
typedef __attribute__((ext_vector_type(4))) float f32x4;
typedef __attribute__((ext_vector_type(16))) float f32x16;
typedef __attribute__((ext_vector_type(4))) unsigned u32x4;
typedef __attribute__((ext_vector_type(2))) unsigned u32x2;

namespace tb {
constexpr double PI = 3.14159265358979323846264338327950288;
constexpr double cos_small(double x) {
  double x2 = x * x, t = 1.0, s = 1.0;
  for (int i = 1; i <= 12; ++i) { t *= -x2 / ((2 * i - 1) * (2 * i)); s += t; }
  return s;
}
constexpr double sin_small(double x) {
  double x2 = x * x, t = x, s = x;
  for (int i = 1; i <= 12; ++i) { t *= -x2 / ((2 * i) * (2 * i + 1)); s += t; }
  return s;
}
constexpr double cos512(int i) {
  const int n = 512;
  i &= 511;
  if (i > n / 2) i = n - i;
  bool neg = false;
  if (i > n / 4) { i = n / 2 - i; neg = true; }
  double v = (i == n / 4) ? 0.0 : (i <= n / 8) ? cos_small(2 * PI * i / n) : sin_small(2 * PI * (n / 4 - i) / n);
  return neg ? -v : v;
}
constexpr u16 f2bf(double d) {
  float f = (float)d;
  unsigned u = __builtin_bit_cast(unsigned, f);
  u += 0x7fffu + ((u >> 16) & 1u);
  return (u16)(u >> 16);
}
constexpr u16 f2h(double d) {
  float f = (float)d;
  unsigned u = __builtin_bit_cast(unsigned, f);
  unsigned sign = (u >> 16) & 0x8000u;
  u &= 0x7fffffffu;
  if (u == 0) return (u16)sign;
  u += 0xfffu + ((u >> 13) & 1u);
  unsigned e = (u >> 23) - 112u, m = (u >> 13) & 0x3ffu;
  return (u16)(sign | (e << 10) | m);
}
struct Cos512 { u16 v[512]; };
constexpr Cos512 mk_cos_bf() { Cos512 c{}; for (int i = 0; i < 512; ++i) c.v[i] = f2bf(cos512(i)); return c; }
constexpr Cos512 mk_cos_h8() { Cos512 c{}; for (int i = 0; i < 512; ++i) c.v[i] = f2h(cos512(i) * 0.125); return c; }
constexpr Cos512 CB = mk_cos_bf();
constexpr Cos512 CH8 = mk_cos_h8();
struct Chunk { u16 v[16384]; };
constexpr Chunk mkF(int ci) {
  Chunk o{};
  const int which = ci >> 3, d0 = (ci & 7) * 64;
  for (int d = 0; d < 64; ++d) {
    const int dd = d0 + d;
    int idx = which ? 128 : 0;
    u16* p = o.v + d * 256;
    for (int c = 0; c < 256; ++c) { p[c] = CB.v[idx & 511]; idx += dd; }
    if (which) p[0] = CB.v[(dd & 1) * 256];
  }
  return o;
}
constexpr Chunk mkT64() {
  Chunk o{};
  for (int w = 0; w < 4; ++w)
    for (int ro = 0; ro < 2; ++ro)
      for (int s = 0; s < 4; ++s)
        for (int l = 0; l < 64; ++l)
          for (int e = 0; e < 8; ++e) {
            const int a = 16 * w + (l & 15), b = 16 * s + 4 * (l >> 4) + (e >> 1), ri = e & 1;
            const int th = 8 * a * b;
            int idx = th;
            if (ro == 0 && ri == 1) idx = th - 128;
            if (ro == 1 && ri == 0) idx = th + 128;
            o.v[((((w * 2 + ro) * 4 + s) * 64 + l) * 8) + e] = CH8.v[idx & 511];
          }
  return o;
}
struct Small { u16 v[2048]; };
constexpr Small mkT32() {
  Small o{};
  for (int cs = 0; cs < 2; ++cs)
    for (int u = 0; u < 2; ++u)
      for (int l = 0; l < 64; ++l)
        for (int j = 0; j < 8; ++j) {
          const int i = l & 15;
          const int a = 8 * (i >> 2) + 4 * u + (i & 3), b = 8 * (l >> 4) + j;
          const int th = 16 * a * b;
          o.v[(((cs * 2 + u) * 64 + l) * 8) + j] = CH8.v[(th - (cs ? 128 : 0)) & 511];
        }
  return o;
}
template <int I> struct FC { static constexpr Chunk v = mkF(I); };
struct Tables { Chunk F[16]; Chunk T64; Small T32; };
}
#define F_(i) tb::FC<i>::v
#if defined(__HIP_DEVICE_COMPILE__)
static __device__ __attribute__((aligned(256))) const tb::Tables g_tab = {
    {F_(0), F_(1), F_(2), F_(3), F_(4), F_(5), F_(6), F_(7), F_(8), F_(9), F_(10), F_(11), F_(12), F_(13), F_(14), F_(15)},
    tb::mkT64(), tb::mkT32()};
#else
static __device__ __attribute__((aligned(256))) const tb::Tables g_tab = {};
#endif

constexpr unsigned CS_ = 8448;
constexpr unsigned YP_ = 4224;
constexpr unsigned WS_SYNC = 0;
constexpr unsigned WS_F1 = 256;
constexpr unsigned WS_F2 = WS_F1 + 2048;
constexpr unsigned WS_F0 = WS_F2 + 2048;
constexpr unsigned WS_FW = WS_F0 + 2048;
constexpr unsigned WS_FX = WS_FW + 2048;
constexpr unsigned WS_RS = 16384;
constexpr unsigned WS_CS = WS_RS + 2048 * 4;
constexpr unsigned WS_X1 = 327680;
constexpr unsigned WS_WT = WS_X1 + 256 * CS_;
constexpr unsigned WS_Y1 = WS_WT + 260 * CS_;
constexpr unsigned WS_H = WS_Y1 + 512 * YP_;
constexpr unsigned WS_END = WS_H + 64 * 112 * 1024;
constexpr size_t WS_DBG = 64ull << 20;
#define STAMP(i)

struct Params {
  const char* x; const char* wr; const char* wi; const char* wg; const char* wu; const char* wd;
  char* out; char* ws;
};
typedef unsigned long long u64;
typedef __amdgpu_buffer_rsrc_t rsrc_t;
constexpr int SC1 = 16;

DI void glds16(unsigned voff, const void* sbase, unsigned ldsdst) {
  asm volatile("s_mov_b32 m0, %2\n\ts_nop 0\n\tglobal_load_lds_dwordx4 %0, %1" ::"v"(voff), "s"(sbase), "s"(ldsdst) : "memory");
}
DI void gload16(u32x4& dst, unsigned voff, const void* sbase) {
  asm volatile("global_load_dwordx4 %0, %1, %2" : "=v"(dst) : "v"(voff), "s"(sbase) : "memory");
}
DI void gload16_sc1(u32x4& dst, unsigned voff, const void* sbase) {
  asm volatile("global_load_dwordx4 %0, %1, %2 sc1" : "=v"(dst) : "v"(voff), "s"(sbase) : "memory");
}
DI void gload8(u32x2& dst, unsigned voff, const void* sbase) {
  asm volatile("global_load_dwordx2 %0, %1, %2" : "=v"(dst) : "v"(voff), "s"(sbase) : "memory");
}
DI void gload8_sc1(u32x2& dst, unsigned voff, const void* sbase) {
  asm volatile("global_load_dwordx2 %0, %1, %2 sc1" : "=v"(dst) : "v"(voff), "s"(sbase) : "memory");
}
template <int N> DI void wait_vm() { asm volatile("s_waitcnt vmcnt(%0)" ::"i"(N) : "memory"); }
template <int N> DI void wait_vm2(u32x4& a, u32x4& b) { asm volatile("s_waitcnt vmcnt(%2)" : "+v"(a), "+v"(b) : "i"(N) : "memory"); }
template <int N> DI void wait_lgkm() { asm volatile("s_waitcnt lgkmcnt(%0)" ::"i"(N) : "memory"); }
DI void bar() { asm volatile("s_barrier" ::: "memory"); }
#define SCHED_FENCE() __builtin_amdgcn_sched_barrier(0)
DI bf16x8 lds_rd128(unsigned a) { return *(const LAS bf16x8*)a; }
DI bf16x8 lds_tr2(unsigned a0, unsigned a1) {
  bf16x4 lo = __builtin_amdgcn_ds_read_tr16_b64_v4bf16((LAS bf16x4*)a0);
  bf16x4 hi = __builtin_amdgcn_ds_read_tr16_b64_v4bf16((LAS bf16x4*)a1);
  return __builtin_shufflevector(lo, hi, 0, 1, 2, 3, 4, 5, 6, 7);
}
DI float bfr(float v) { return (float)(__bf16)v; }
DI unsigned pk_bf(float a, float b) { f32x2 v = {a, b}; return __builtin_bit_cast(unsigned, __builtin_convertvector(v, bf16x2)); }
DI unsigned pk_h(float a, float b) { f32x2 v = {a, b}; return __builtin_bit_cast(unsigned, __builtin_convertvector(v, f16x2)); }
DI float bf_lo(unsigned u) { return __builtin_bit_cast(float, u << 16); }
DI float bf_hi(unsigned u) { return __builtin_bit_cast(float, u & 0xffff0000u); }
DI unsigned fbits(float f) { return __builtin_bit_cast(unsigned, f); }
DI float bitsf(unsigned u) { return __builtin_bit_cast(float, u); }
template <int CTRL, int RM> DI float dpp0(float v) {
  return __builtin_bit_cast(float, __builtin_amdgcn_update_dpp(0, __builtin_bit_cast(int, v), CTRL, RM, 0xf, RM == 0xf));
}
DI float sum8(float v) { v += dpp0<0x111, 0xf>(v); v += dpp0<0x112, 0xf>(v); v += dpp0<0x114, 0xf>(v); return v; }
DI float sum32(float v) { v = sum8(v); v += dpp0<0x118, 0xf>(v); v += dpp0<0x142, 0xa>(v); return v; }
DI float sum64u(float v) { v = sum32(v); v += dpp0<0x143, 0xc>(v); return __builtin_bit_cast(float, __builtin_amdgcn_readlane(__builtin_bit_cast(int, v), 63)); }
template <int CTRL, int RM> DI unsigned dppu(unsigned v) { return (unsigned)__builtin_amdgcn_update_dpp(0, (int)v, CTRL, RM, 0xf, RM == 0xf); }
DI unsigned max64u(unsigned v) {
  v = max(v, dppu<0x111, 0xf>(v)); v = max(v, dppu<0x112, 0xf>(v)); v = max(v, dppu<0x114, 0xf>(v)); v = max(v, dppu<0x118, 0xf>(v));
  v = max(v, dppu<0x142, 0xa>(v)); v = max(v, dppu<0x143, 0xc>(v));
  return (unsigned)__builtin_amdgcn_readlane((int)v, 63);
}
#define MFMA32(a, b, c) __builtin_amdgcn_mfma_f32_32x32x16_bf16((a), (b), (c), 0, 0, 0)
#define MFMA16H(a, b, c) __builtin_amdgcn_mfma_f32_16x16x32_f16((a), (b), (c), 0, 0, 0)
DI f16x8 pack8h(const f32x4& a, const f32x4& b) {
  u32x4 o = {pk_h(a[0], a[1]), pk_h(a[2], a[3]), pk_h(b[0], b[1]), pk_h(b[2], b[3])};
  return __builtin_bit_cast(f16x8, o);
}
DI f16x8 negh(const f16x8& a) { u32x4 o = __builtin_bit_cast(u32x4, a); o ^= 0x80008000u; return __builtin_bit_cast(f16x8, o); }

constexpr unsigned SPIN_CAP = 1u << 15;
#define G_DEPTH 8
constexpr unsigned L_STAGE = 49152;
constexpr unsigned wd_slot(int k) { return k == 0 ? 98304u : k == 1 ? 114688u : k == 2 ? 131072u : k == 3 ? 0u : k == 4 ? 16384u : k == 5 ? 32768u : k == 6 ? 49152u : 65536u; }
constexpr unsigned L_CREG = 81920;
constexpr unsigned L_CREG2 = 147456;
constexpr unsigned L_MISC = 155648;
constexpr unsigned L_CTR = L_MISC, L_F = L_MISC + 5120, L_C = L_MISC + 6144, L_MX = L_MISC + 96, L_TAG = L_MISC + 128, L_STAMP = L_MISC + 4096, L_ST = L_MISC + 1024, L_CSF = L_MISC + 2048;
constexpr unsigned C_H = L_CTR, C_Y = L_CTR + 4, C_EX = L_CTR + 8, C_ST = L_CTR + 12, C_O = L_CTR + 16, C_CS = L_CTR + 20, C_F0 = L_CTR + 24,
                   C_WD = L_CTR + 28, C_WT = L_CTR + 32, R_H = L_CTR + 36, R_X = L_CTR + 40, R_Y = L_CTR + 44, R_S = L_CTR + 48;
constexpr unsigned LDS_BYTES = 160 * 1024;

DI unsigned opaque(unsigned v) { asm volatile("" : "+v"(v)); return v; }
DI void glds4(const unsigned (&vo)[4], const void* sbase, unsigned ldsdst) {
  asm volatile("s_mov_b32 m0, %5\n\ts_nop 0\n\tglobal_load_lds_dwordx4 %0, %4\n\tglobal_load_lds_dwordx4 %1, %4 offset:1024\n\t"
               "global_load_lds_dwordx4 %2, %4 offset:2048\n\tglobal_load_lds_dwordx4 %3, %4 offset:3072"
               ::"v"(vo[0]), "v"(vo[1]), "v"(vo[2]), "v"(vo[3]), "s"(sbase), "s"(ldsdst) : "memory");
}
DI unsigned lds_ld(unsigned a) { return __builtin_amdgcn_readfirstlane(__hip_atomic_load((LAS unsigned*)a, __ATOMIC_RELAXED, __HIP_MEMORY_SCOPE_WORKGROUP)); }
DI unsigned lds_arrive(unsigned ctr, int l) {
  unsigned old = 0;
  if (l == 0) old = __hip_atomic_fetch_add((LAS unsigned*)ctr, 1u, __ATOMIC_RELAXED, __HIP_MEMORY_SCOPE_WORKGROUP);
  return old;
}
DI void lds_wait(unsigned ctr, unsigned target) {
#pragma unroll 1
  for (unsigned s = 0; s < (SPIN_CAP << 4); ++s) { if (lds_ld(ctr) >= target) break; __builtin_amdgcn_s_sleep(1); }
  asm volatile("" ::: "memory");
}
DI void lds_set(unsigned a) { if ((threadIdx.x & 63) == 0) *(volatile LAS unsigned*)a = 1u; }
template <int SLEEP> DI void poll1(rsrc_t rs, unsigned off, unsigned lo, unsigned hi) {
  bool ok = false;
#pragma unroll 1
  for (unsigned s = 0; s < SPIN_CAP; ++s) {
    if (!ok) { const u32x2 f = __builtin_amdgcn_raw_buffer_load_b64(rs, off, 0, SC1); ok = f[0] == lo && f[1] == hi; }
    if (__all(ok)) break;
    __builtin_amdgcn_s_sleep(SLEEP);
  }
  asm volatile("" ::: "memory");
}
template <int SLEEP> DI void poll4(rsrc_t rs, unsigned off, unsigned lo, unsigned hi) {
  bool ok = false;
#pragma unroll 1
  for (unsigned s = 0; s < SPIN_CAP; ++s) {
    if (!ok) {
      const u32x4 f = __builtin_amdgcn_raw_buffer_load_b128(rs, off, 0, SC1), g = __builtin_amdgcn_raw_buffer_load_b128(rs, off + 16, 0, SC1);
      ok = ((f[0] ^ lo) | (f[1] ^ hi) | (f[2] ^ lo) | (f[3] ^ hi) | (g[0] ^ lo) | (g[1] ^ hi) | (g[2] ^ lo) | (g[3] ^ hi)) == 0;
    }
    if (__all(ok)) break;
    __builtin_amdgcn_s_sleep(SLEEP);
  }
  asm volatile("" ::: "memory");
}
DI void sync4(unsigned ctr, unsigned target, int l) {
  wait_lgkm<0>();
  lds_arrive(ctr, l);
  lds_wait(ctr, target);
}
DI void gload_u16(unsigned& dst, unsigned voff, const void* sbase) {
  asm volatile("global_load_ushort %0, %1, %2" : "=v"(dst) : "v"(voff), "s"(sbase) : "memory");
}
constexpr int GW = 16, GH = G_DEPTH;
constexpr int g2_allow(int i) {
  int seq_w[28] = {}, seq_h[28] = {}, n = 0;
  for (int k = 0; k < GH; ++k) { n += 2; seq_h[k] = n; }
  for (int s = 0; s < i; ++s) {
    if (s + GW < 28) { n += 2; seq_w[s + GW] = n; }
    if (s + GH < 28) { n += 2; seq_h[s + GH] = n; }
  }
  int need = seq_h[i];
  if (i + 1 < 28 && seq_w[i + 1] > need) need = seq_w[i + 1];
  return n - need;
}

__global__ __launch_bounds__(512, 2) void fused_kernel(Params P) {
  const int tid = threadIdx.x, l = tid & 63, b = blockIdx.x;
  const int w = __builtin_amdgcn_readfirstlane(tid >> 6);
  const int h = l >> 5, w4 = w & 3;
  const rsrc_t rs = __builtin_amdgcn_make_buffer_rsrc((void*)P.ws, 0, (int)WS_END, 0x00020000);
  const int xcd = b & 7, idx = b >> 3;
  const int p = (xcd & 1) * 8 + (idx & 7), j = (xcd >> 1) * 4 + (idx >> 3);
  const int r64 = xcd * 4 + (idx & 3), t64 = idx >> 2;
  const bool dft = j >= 14;
  const unsigned swz = ((l & 7) ^ (((l >> 4) & 1) << 2)) << 4;
  const unsigned wdo = (l >> 3) * 1024 + swz;
  const int blk = (l >> 4) & 1, q4 = (l & 15) >> 2, pp = l & 3;
  const char* wds = P.wd + (size_t)(448 * w4) * 1024 + t64 * 128;
  const unsigned rot2 = (unsigned)((7 * t64 + 14 * (r64 & 3)) >> 2) % 14u;
  unsigned tag_lo, tag_hi;
  STAMP(0);

  if (w < 4) {
    {
      if (tid < 6) *(LAS u32x4*)(tid < 4 ? L_CTR + tid * 16 : tid == 4 ? L_F : L_C) = u32x4{0u, 0u, 0u, 0u};
      wait_lgkm<0>(); bar();
      unsigned xa[4], wa[2];
#pragma unroll
      for (int k4 = 0; k4 < 4; ++k4) xa[k4] = 16384 + (l & 31) * 128 + (((2 * k4 + h) ^ ((l >> 1) & 7)) << 4);
#pragma unroll
      for (int tt = 0; tt < 2; ++tt)
        wa[tt] = blk * 8192 + (8 * h + q4) * 256 + (((4 * w4 + 2 * tt + (pp >> 1)) ^ ((q4 << 2) | (blk << 1))) << 4) + (pp & 1) * 8;
      f32x16 acc[4][2];
#pragma unroll
      for (int a = 0; a < 4; ++a)
#pragma unroll
        for (int t = 0; t < 2; ++t)
#pragma unroll
          for (int r = 0; r < 16; ++r) acc[a][t][r] = 0.f;
      bf16x8 xfA[4], wfA[2], xfB[4], wfB[2];
      auto load_frags = [&](unsigned so, int sub, int ks, bf16x8(&xfr)[4], bf16x8(&wfr)[2]) {
#pragma unroll
        for (int tt = 0; tt < 2; ++tt) wfr[tt] = lds_tr2(wa[tt] + so + sub * 32768 + ks * 4096, wa[tt] + so + sub * 32768 + ks * 4096 + 1024);
#pragma unroll
        for (int a = 0; a < 4; ++a) xfr[a] = lds_rd128(xa[sub * 2 + ks] + so + a * 4096);
      };
      auto compute = [&](bf16x8(&xfr)[4], bf16x8(&wfr)[2]) {
#pragma unroll
        for (int a = 0; a < 4; ++a)
#pragma unroll
          for (int tt = 0; tt < 2; ++tt) {
            acc[a][tt] = MFMA32(wfr[tt], xfr[a], acc[a][tt]);
          }
      };
      auto interleave = [&]() {
#pragma unroll
        for (int i = 0; i < 5; ++i) {
          __builtin_amdgcn_sched_group_barrier(0x008, 1, 0);
          __builtin_amdgcn_sched_group_barrier(0x002, 2, 0);
          __builtin_amdgcn_sched_group_barrier(0x080, 2, 0);
        }
        __builtin_amdgcn_sched_group_barrier(0x008, 3, 0);
        SCHED_FENCE();
      };
      auto flags = [&]() { return *(volatile const LAS u32x2*)L_F; };
      auto landed = [&](u32x2 f, unsigned need) {
#pragma unroll 1
        for (unsigned s = 0; s < (SPIN_CAP << 4); ++s) {
          if (__builtin_amdgcn_readfirstlane(min(f[0], f[1])) >= need) break;
          __builtin_amdgcn_s_sleep(1);
          f = flags();
        }
        asm volatile("" ::: "memory");
      };
      asm volatile("" : "+v"(acc[0][0]), "+v"(acc[0][1]), "+v"(acc[1][0]), "+v"(acc[1][1]), "+v"(acc[2][0]), "+v"(acc[2][1]), "+v"(acc[3][0]), "+v"(acc[3][1]));
      asm volatile("" : "+v"(xa[0]), "+v"(xa[1]), "+v"(xa[2]), "+v"(xa[3]), "+v"(wa[0]), "+v"(wa[1]));
      landed(flags(), 1);
      STAMP(2);
      load_frags(0, 0, 0, xfA, wfA);
      unsigned so = 0;
#pragma unroll 1
      for (unsigned v = 0; v < 8; ++v) {
        const unsigned son = (so == 2 * L_STAGE) ? 0u : so + L_STAGE;
        u32x2 f = flags();
        load_frags(so, 0, 1, xfB, wfB);
        *(volatile LAS unsigned*)(L_C + w4 * 4 + l * 16) = 2 * v + 1;
        compute(xfA, wfA);
        interleave();
        landed(f, 2 * v + 2);
        load_frags(so, 1, 0, xfA, wfA);
        compute(xfB, wfB);
        interleave();
        f = flags();
        load_frags(so, 1, 1, xfB, wfB);
        *(volatile LAS unsigned*)(L_C + w4 * 4 + l * 16) = 2 * v + 2;
        compute(xfA, wfA);
        interleave();
        landed(f, min(2 * v + 3, 16u));
        load_frags(son, 0, 0, xfA, wfA);
        compute(xfB, wfB);
        interleave();
        so = son;
      }
      STAMP(3);
      lds_wait(C_ST, 1);
      tag_lo = lds_ld(L_TAG); tag_hi = lds_ld(L_TAG + 4);
      float rstd[4];
      {
        bf16x8 fa[2], fb[4];
#pragma unroll
        for (int a = 0; a < 4; ++a) {
          const u32x2 st = *(const LAS u32x2*)(L_ST + (a * 32 + (l & 31)) * 8);
          const u32x4 vb = {st[0], st[0] & 0xffffu, 0u, 0u};
          fb[a] = __builtin_bit_cast(bf16x8, vb);
          rstd[a] = bitsf(st[1]);
        }
#pragma unroll
        for (int tt = 0; tt < 2; ++tt) {
          const u32x2 c = *(const LAS u32x2*)(L_CSF + (w4 * 64 + tt * 32 + (l & 31)) * 8);
          const u32x4 va = {h ? 0u : c[0], h ? 0u : c[1], 0u, 0u};
          fa[tt] = __builtin_bit_cast(bf16x8, va);
        }
        compute(fb, fa);
      }
      if (!dft) {
#pragma unroll
        for (int a = 0; a < 4; ++a) {
          const float r2 = rstd[a] * rstd[a];
#pragma unroll
          for (int tt = 0; tt < 2; ++tt) {
            u32x4 o;
#pragma unroll
            for (int i = 0; i < 4; ++i)
              o[i] = pk_bf(fmaxf(acc[a][tt][2 * i], 0.f) * acc[a][tt][8 + 2 * i] * r2, fmaxf(acc[a][tt][2 * i + 1], 0.f) * acc[a][tt][9 + 2 * i] * r2);
            const int rt = p * 4 + a, ks = j * 8 + 2 * w4 + tt;
            __builtin_amdgcn_raw_buffer_store_b128(o, rs, opaque(l * 16 + WS_H + (rt * 112 + ks) * 1024), 0, SC1);
          }
        }
      } else {
        const int n2 = l & 31;
        const unsigned lo = WS_X1 + ((n2 >> 2) & 1) * 4096 + (p >> 2) * 1024 + ((p & 3) * 16 + 4 * (n2 >> 3) + (n2 & 3)) * 16 +
                            ((j - 14) * 128 + w4 * 32 + 4 * h) * CS_;
#pragma unroll
        for (int tt = 0; tt < 2; ++tt)
#pragma unroll
          for (int r = 0; r < 8; ++r) {
            u32x4 o;
#pragma unroll
            for (int a = 0; a < 4; ++a) o[a] = pk_h(acc[a][tt][r] * rstd[a], acc[a][tt][r + 8] * rstd[a]);
            __builtin_amdgcn_raw_buffer_store_b128(o, rs, opaque(lo + (tt * 16 + (r & 3) + 8 * (r >> 2)) * CS_), 0, SC1);
          }
      }
      STAMP(4);
      wait_vm<0>();
      if (lds_arrive(C_H, l) == 3 && l == 0)
        __builtin_amdgcn_raw_buffer_store_b64(u32x2{tag_lo, tag_hi}, rs, dft ? WS_FX + ((j - 14) * 16 + p) * 8 : WS_F1 + (p * 16 + j) * 8, 0, SC1);
      STAMP(5);
    }
    const unsigned ooff = (r64 * 64 + (w4 >> 1) * 32 + (l & 31)) * 1024 + (t64 * 64 + (w4 & 1) * 32 + 4 * h) * 2;
    u32x2 xv[4];
#pragma unroll
    for (int g = 0; g < 4; ++g) gload8(xv[g], ooff + g * 16, P.x);
    f32x16 acc[2][2];
#pragma unroll
    for (int a = 0; a < 2; ++a)
#pragma unroll
      for (int t = 0; t < 2; ++t)
#pragma unroll
        for (int r = 0; r < 16; ++r) acc[a][t][r] = 0.f;
    auto kstep = [&](int i) { const unsigned k = 2 * rot2 + i; return k >= 28 ? k - 28 : k; };
    auto issue_wd = [&](int i) {
      const unsigned dst = wd_slot((i % GW) >> 1) + w4 * 4096 + (i & 1) * 2048;
      const char* src = wds + kstep(i) * 16384;
      glds16(wdo, src, dst);
      glds16(wdo, src + 8192, dst + 1024);
    };
    const char* hs0 = P.ws + WS_H + (size_t)((2 * r64 + (w4 >> 1)) * 112 + 28 * w4) * 1024;
    const char* hs1 = P.ws + WS_H + (size_t)((2 * r64 + ((w4 >> 1) ^ 1)) * 112 + 28 * w4) * 1024;
    const unsigned lo16 = l * 16;
    u32x4 rf[GH][2];
    auto issue_h = [&](int i) {
      gload16_sc1(rf[i % GH][0], lo16, hs0 + kstep(i) * 1024);
      gload16_sc1(rf[i % GH][1], lo16, hs1 + kstep(i) * 1024);
    };
    STAMP(6);
    if (w == 0) { poll1<6>(rs, WS_F1 + ((r64 >> 1) * 16 + (l < 14 ? l : 0)) * 8, tag_lo, tag_hi); lds_set(R_H); }
    else lds_wait(R_H, 1);
    asm volatile("s_waitcnt vmcnt(0)" : "+v"(xv[0]), "+v"(xv[1]), "+v"(xv[2]), "+v"(xv[3])::"memory");
    STAMP(7);
#pragma unroll
    for (int i = 0; i < GH; ++i) issue_h(i);
    lds_wait(C_WD, 2);
    unsigned ta[2];
#pragma unroll
    for (int t = 0; t < 2; ++t)
      ta[t] = w4 * 4096 + (4 * h + q4) * 128 + (((4 * (t ^ (w4 & 1)) + 2 * blk + (pp >> 1)) ^ (((q4 >> 1) & 1) << 2)) << 4) + (pp & 1) * 8;
    bf16x8 lf[2][2];
    auto rd = [&](int i) {
      const unsigned o = wd_slot((i % GW) >> 1) + (i & 1) * 2048;
#pragma unroll
      for (int t = 0; t < 2; ++t) lf[i & 1][t] = lds_tr2(ta[t] + o, ta[t] + o + 1024);
    };
    rd(0);
#define G_STEP(i)                                                                                          \
  {                                                                                                        \
    wait_vm2<g2_allow(i)>(rf[(i) % GH][0], rf[(i) % GH][1]);                                                \
    if ((i) < 27) rd((i) + 1);                                                                              \
    SCHED_FENCE();                                                                                          \
    const bf16x8 r0 = __builtin_bit_cast(bf16x8, rf[(i) % GH][0]), r1 = __builtin_bit_cast(bf16x8, rf[(i) % GH][1]); \
    acc[0][0] = MFMA32(lf[(i) & 1][0], r0, acc[0][0]); acc[0][1] = MFMA32(lf[(i) & 1][1], r0, acc[0][1]);    \
    acc[1][0] = MFMA32(lf[(i) & 1][0], r1, acc[1][0]); acc[1][1] = MFMA32(lf[(i) & 1][1], r1, acc[1][1]);    \
    SCHED_FENCE();                                                                                          \
    if ((i) + GW < 28) issue_wd((i) + GW);                                                                  \
    if ((i) + GH < 28) issue_h((i) + GH);                                                                   \
  }
    G_STEP(0) G_STEP(1) G_STEP(2) G_STEP(3) G_STEP(4) G_STEP(5) G_STEP(6) G_STEP(7) G_STEP(8) G_STEP(9)
    G_STEP(10) G_STEP(11) G_STEP(12) G_STEP(13) G_STEP(14) G_STEP(15) G_STEP(16) G_STEP(17) G_STEP(18)
    G_STEP(19) G_STEP(20) G_STEP(21) G_STEP(22) G_STEP(23) G_STEP(24) G_STEP(25) G_STEP(26) G_STEP(27)
#undef G_STEP
    STAMP(8);
#pragma unroll
    for (int jj = 1; jj < 4; ++jj)
#pragma unroll
      for (int g = 0; g < 4; ++g) {
        const f32x16& o = acc[jj >> 1][jj & 1];
        *(LAS f32x4*)(wd_slot(jj) + w4 * 4096 + g * 1024 + lo16) = f32x4{o[4 * g], o[4 * g + 1], o[4 * g + 2], o[4 * g + 3]};
      }
    sync4(C_O, 4, l);
#pragma unroll
    for (int jj = 1; jj < 4; ++jj)
#pragma unroll
      for (int g = 0; g < 4; ++g) {
        const f32x4 o = *(const LAS f32x4*)(wd_slot(jj) + (w4 ^ jj) * 4096 + g * 1024 + lo16);
#pragma unroll
        for (int e = 0; e < 4; ++e) acc[0][0][4 * g + e] += o[e];
      }
#pragma unroll
    for (int r = 0; r < 16; ++r) {
      const float ob = bfr(acc[0][0][r]);
      acc[0][0][r] = bfr(ob * __builtin_amdgcn_rcpf(1.f + __expf(-ob)));
    }
    STAMP(9);
    bar();
    STAMP(12);
#pragma unroll
    for (int g = 0; g < 4; ++g) {
      const unsigned ss = wd_slot(4) + w4 * 16384 + g * 1024 + lo16;
      const f32x4 s = (*(const LAS f32x4*)ss + *(const LAS f32x4*)(ss + 4096)) + (*(const LAS f32x4*)(ss + 8192) + *(const LAS f32x4*)(ss + 12288));
      float r[4];
#pragma unroll
      for (int e = 0; e < 4; ++e) {
        const float sg = bfr(bfr(s[e]) * acc[0][0][4 * g + e]);
        r[e] = ((e & 1) ? bf_hi(xv[g][e >> 1]) : bf_lo(xv[g][e >> 1])) + sg;
      }
      *(u32x2*)(P.out + ooff + g * 16) = u32x2{pk_bf(r[0], r[1]), pk_bf(r[2], r[3])};
    }
  } else {
    const char* tabF = (const char*)&g_tab.F[0];
    const char* m1 = dft ? tabF + (j - 14) * 256 : P.wg + j * 256;
    const char* m2 = dft ? tabF + 262144 + (j - 14) * 256 : P.wu + j * 256;
    const unsigned ld = dft ? 512u : 3584u;
    const int hw = w & 1;
    if (w < 6) {
      __builtin_amdgcn_s_setprio(3);
      const char* xs = P.x + (size_t)(p * 128 + hw * 64) * 1024;
      m1 += (size_t)(16 * hw) * ld; m2 += (size_t)(16 * hw) * ld;
      const char* wdp = P.wd + (size_t)(896 * hw) * 1024 + t64 * 128;
      unsigned xo[4], wo0[4], wo1[4], dq[4];
#pragma unroll
      for (int i = 0; i < 4; ++i) {
        xo[i] = (l >> 3) * 1024 + (((l & 7) ^ ((i & 1) * 4 + (l >> 4))) << 4) + i * (8192 - 1024);
        wo0[i] = (l >> 4) * ld + (((l & 15) ^ ((l >> 4) << 2)) << 4) + i * (4 * ld - 1024);
        wo1[i] = (l >> 4) * ld + (((l & 15) ^ (((l >> 4) << 2) | 2)) << 4) + i * (4 * ld - 1024);
        dq[i] = wdo + i * (8192 - 1024);
      }
      unsigned kx = 0, kw = 0, kd = rot2;
      auto issue_x = [&](unsigned so) {
        const unsigned d = so + 16384 + hw * 8192;
        glds4(xo, xs + kx * 128, d); glds4(xo, xs + kx * 128 + 32768, d + 4096);
        kx = (kx + 1) & 7;
      };
      auto issue_w = [&](unsigned so) {
        const unsigned d = so + hw * 4096;
        glds4(wo0, m1 + kw * 32 * ld, d); glds4(wo1, m2 + kw * 32 * ld, d + 8192);
        kw = (kw + 1) & 15;
      };
      auto issue_d = [&](unsigned so) {
        const unsigned d = so + hw * 8192;
        glds4(dq, wdp + kd * 32768, d); glds4(dq, wdp + kd * 32768 + 448 * 1024, d + 4096);
        kd = kd == 13 ? 0 : kd + 1;
      };
      auto publish = [&](unsigned n) { *(volatile LAS unsigned*)(L_F + hw * 4 + l * 16) = n; };
      auto consumed = [&](unsigned need) {
#pragma unroll 1
        for (unsigned s = 0; s < (SPIN_CAP << 4); ++s) {
          const u32x4 c = *(volatile const LAS u32x4*)L_C;
          if (__builtin_amdgcn_readfirstlane(min(min(c[0], c[1]), min(c[2], c[3]))) >= need) break;
          __builtin_amdgcn_s_sleep(1);
        }
        asm volatile("" ::: "memory");
      };
      issue_w(0); issue_x(0);
      bar();
      issue_w(32768);
      STAMP(1);
      wait_vm<8>(); publish(1);
      STAMP(2);
      issue_w(L_STAGE);
      issue_x(L_STAGE); wait_vm<16>(); publish(2);
      issue_w(L_STAGE + 32768);
      issue_w(2 * L_STAGE); wait_vm<16>(); publish(3);
      issue_x(2 * L_STAGE); wait_vm<16>(); publish(4);
      issue_w(2 * L_STAGE + 32768);
      unsigned so = 0;
#pragma unroll 1
      for (unsigned v = 0; v < 8; ++v) {
        consumed(2 * v + 1);
        if (v < 5) issue_w(so); else issue_d(so);
        wait_vm<16>(); publish(2 * v + 5);
        consumed(2 * v + 2);
        if (v < 5) issue_x(so); else issue_d(so + 16384);
        wait_vm<16>(); publish(2 * v + 6);
        if (v < 5) issue_w(so + 32768); else if (v < 7) issue_d(so + 32768);
        so = (so == 2 * L_STAGE) ? 0u : so + L_STAGE;
      }
      STAMP(3);
      wait_vm<0>();
      lds_arrive(C_WD, l);
      lds_wait(C_ST, 1);
      tag_lo = lds_ld(L_TAG); tag_hi = lds_ld(L_TAG + 4);
    } else {
      __builtin_amdgcn_s_setprio(2);
      const u32x2 ep = __builtin_amdgcn_raw_buffer_load_b64(rs, WS_SYNC, 0, SC1);
      u32x4 sv[24];
      unsigned vxr = 0, vxi = 0;
      const int xc = min(l >> 3, 2);
      if (hw == 0) {
        const char* xr = P.x + (size_t)(p * 128 + j * 8) * 1024 + l * 16;
#pragma unroll
        for (int r = 0; r < 8; ++r) sv[r] = *(const u32x4*)(xr + r * 1024);
        const char* csb = ((p & 2) ? m2 : m1) + (p & 1) * 128 + (size_t)((p >> 2) * 128 + (l >> 3)) * ld + (l & 7) * 16;
#pragma unroll
        for (int i = 0; i < 16; ++i) sv[8 + i] = *(const u32x4*)(csb + (size_t)(8 * i) * ld);
      }
      bar();
      {
        u64 e = ((u64)(unsigned)__builtin_amdgcn_readfirstlane(ep[1]) << 32) | (unsigned)__builtin_amdgcn_readfirstlane(ep[0]);
        e += 0x9E3779B97F4A7C15ull;
        e = (e ^ (e >> 30)) * 0xBF58476D1CE4E5B9ull;
        e = (e ^ (e >> 27)) * 0x94D049BB133111EBull;
        e ^= e >> 31;
        tag_lo = (unsigned)e; tag_hi = (unsigned)(e >> 32);
      }
      STAMP(16);
      if (hw == 0) {
        if (l == 0) *(LAS u32x2*)L_TAG = u32x2{tag_lo, tag_hi};
        float mm = 0.f, vv = 0.f;
#pragma unroll
        for (int r = 0; r < 8; ++r) {
          float xv[8];
#pragma unroll
          for (int i = 0; i < 4; ++i) { xv[2 * i] = bf_lo(sv[r][i]); xv[2 * i + 1] = bf_hi(sv[r][i]); }
          const float s = ((xv[0] + xv[1]) + (xv[2] + xv[3])) + ((xv[4] + xv[5]) + (xv[6] + xv[7]));
          const float mean = sum64u(s) * (1.f / 512.f);
          float qv = 0.f;
#pragma unroll
          for (int i = 0; i < 8; ++i) { const float d = xv[i] - mean; qv += d * d; }
          const float var = sum64u(qv) * (1.f / 512.f);
          if (l == r) { mm = mean; vv = var; }
        }
        const float sd = bfr(sqrtf(bfr(bfr(vv) + 1e-5f)));
        if (l < 8) __builtin_amdgcn_raw_buffer_store_b32((fbits(bfr(mm)) >> 16) | fbits(sd), rs, WS_RS + (p * 128 + j * 8 + l) * 4, 0, SC1);
        f32x4 ca = {0.f, 0.f, 0.f, 0.f}, cb = ca;
#pragma unroll
        for (int k = 0; k < 16; ++k)
#pragma unroll
          for (int i = 0; i < 2; ++i) {
            ca[2 * i] += bf_lo(sv[8 + k][i]); ca[2 * i + 1] += bf_hi(sv[8 + k][i]);
            cb[2 * i] += bf_lo(sv[8 + k][i + 2]); cb[2 * i + 1] += bf_hi(sv[8 + k][i + 2]);
          }
        *(LAS f32x4*)(L_CREG2 + l * 32) = ca;
        *(LAS f32x4*)(L_CREG2 + l * 32 + 16) = cb;
        float s = 0.f;
#pragma unroll
        for (int i = 0; i < 8; ++i) s += *(const LAS float*)(L_CREG2 + i * 256 + l * 4);
        const int ci = ((p & 1) * 4 + (l >> 4)) * 32 + ((p >> 1) & 1) * 16 + (l & 15);
        __builtin_amdgcn_raw_buffer_store_b32(fbits(s), rs, WS_CS + ((j * 4 + (p >> 2)) * 256 + ci) * 4, 0, SC1);
        STAMP(17);
        wait_vm<0>();
        if (l == 0) __builtin_amdgcn_raw_buffer_store_b64(u32x2{tag_lo, tag_hi}, rs, WS_F0 + (p * 16 + j) * 8, 0, SC1);
        STAMP(18);
        __builtin_amdgcn_s_sleep(16);
        poll1<8>(rs, WS_F0 + ((l & 16) ? (l & 15) * 16 + j : p * 16 + (l & 15)) * 8, tag_lo, tag_hi);
        STAMP(21);
        const u32x2 rsv = __builtin_amdgcn_raw_buffer_load_b64(rs, WS_RS + (p * 128 + 2 * l) * 4, 0, SC1);
        u32x4 cv[4];
#pragma unroll
        for (int k = 0; k < 4; ++k) cv[k] = __builtin_amdgcn_raw_buffer_load_b128(rs, WS_CS + (j * 4 + k) * 1024 + l * 16, 0, SC1);
        const unsigned nm0 = (rsv[0] & 0xffffu) ^ 0x8000u, nm1 = (rsv[1] & 0xffffu) ^ 0x8000u;
        *(LAS u32x4*)(L_ST + l * 16) = u32x4{nm0 | (nm0 << 16), fbits(1.f / bf_hi(rsv[0])), nm1 | (nm1 << 16), fbits(1.f / bf_hi(rsv[1]))};
#pragma unroll
        for (int i = 0; i < 4; ++i) {
          const float c = (bitsf(cv[0][i]) + bitsf(cv[1][i])) + (bitsf(cv[2][i]) + bitsf(cv[3][i]));
          const float c0 = bf_hi(fbits(c)), r1 = c - c0, c1 = bf_hi(fbits(r1)), c2 = r1 - c1;
          *(LAS u32x2*)(L_CSF + l * 32 + i * 8) = u32x2{(fbits(c0) >> 16) | fbits(c1), fbits(c2) >> 16};
        }
        wait_lgkm<0>();
        lds_set(C_ST);
        STAMP(22);
      } else {
        {
          const unsigned o = ((16 * (p >> 2) + (l & 15) + 512 * (p & 3) + 256 * h) * 257 + 16 * j + 8 * blk) * 2;
#pragma unroll
          for (int e = 0; e < 4; ++e) { gload16(sv[e], o, P.wr + e * 32896); gload16(sv[4 + e], o, P.wi + e * 32896); }
          int k = 16 * (b >> 6) + (b & 15) + 64 * (8 * ((b >> 4) & 3) + (l & 7));
          if (xc) k = (2048 - k) & 2047;
          const unsigned o2 = (k * 257 + (xc == 1 ? 0 : 256)) * 2;
          gload_u16(vxr, o2, P.wr); gload_u16(vxi, o2, P.wi);
          asm volatile("s_waitcnt vmcnt(0)" : "+v"(sv[0]), "+v"(sv[1]), "+v"(sv[2]), "+v"(sv[3]), "+v"(sv[4]), "+v"(sv[5]), "+v"(sv[6]), "+v"(sv[7]), "+v"(vxr), "+v"(vxi)::"memory");
        }
        STAMP(17);
        const unsigned d = WS_WT + (16 * j + 8 * blk) * CS_ + (p >> 2) * 2048 + ((p & 3) * 16 + (l & 15)) * 32 + h * 16;
#pragma unroll
        for (int c = 0; c < 8; ++c) {
          u32x4 o;
#pragma unroll
          for (int e = 0; e < 4; ++e) o[e] = __builtin_amdgcn_perm(sv[4 + e][c >> 1], sv[e][c >> 1], (c & 1) ? 0x07060302u : 0x05040100u);
          __builtin_amdgcn_raw_buffer_store_b128(o, rs, opaque(d + c * CS_), 0, SC1);
        }
        if (l < 24) __builtin_amdgcn_raw_buffer_store_b32(vxr | (vxi << 16), rs, WS_WT + (256 + xc) * CS_ + b * 32 + (l & 7) * 4, 0, SC1);
        wait_vm<0>();
        if (l == 0) __builtin_amdgcn_raw_buffer_store_b64(u32x2{tag_lo, tag_hi}, rs, WS_FW + (j * 16 + p) * 8, 0, SC1);
        STAMP(18);
      }
    }
    f16x8 c64[2][4], c32[2][2];
    {
      const char* t64p = (const char*)&g_tab.T64 + w4 * 8192 + l * 16;
      const char* t32p = (const char*)&g_tab.T32 + l * 16;
#pragma unroll
      for (int i = 0; i < 8; ++i) c64[i >> 2][i & 3] = *(const f16x8*)(t64p + i * 1024);
#pragma unroll
      for (int i = 0; i < 4; ++i) c32[i >> 1][i & 1] = *(const f16x8*)(t32p + i * 1024);
    }
    STAMP(5);
    __builtin_amdgcn_s_setprio(2);
    {
      const int q = l >> 4;
      float twc[8], tws[8];
      const int k1 = 16 * w4 + (l & 15);
#pragma unroll
      for (int jx = 0; jx < 8; ++jx) {
        const float a = (float)((8 * q + jx) * k1) * (1.f / 2048.f);
        twc[jx] = __builtin_amdgcn_cosf(a); tws[jx] = __builtin_amdgcn_sinf(a);
      }
#pragma unroll
      for (int jx = 0; jx < 8; ++jx) asm volatile("" : "+v"(twc[jx]), "+v"(tws[jx]));
#pragma unroll
      for (int i = 0; i < 4; ++i) asm volatile("" : "+v"(c64[0][i]), "+v"(c64[1][i]), "+v"(c32[i >> 1][i & 1]));
      lds_wait(C_H, 4);
      STAMP(6);
      if (w == 6) {
        if (b == 0) poll4<4>(rs, WS_FW + l * 32, tag_lo, tag_hi);
        poll1<4>(rs, ((l & 16) ? WS_FX + (b >> 7) * 128 : WS_FW + (b >> 4) * 128) + (l & 15) * 8, tag_lo, tag_hi);
        lds_set(R_X);
      } else lds_wait(R_X, 1);
      STAMP(7);
      u32x4 zr[2][4];
#pragma unroll
      for (int t = 0; t < 2; ++t)
#pragma unroll
        for (int s = 0; s < 4; ++s) zr[t][s] = __builtin_amdgcn_raw_buffer_load_b128(rs, l * 16 + t * 4096 + s * 1024, WS_X1 + b * CS_, SC1);
      const f32x4 zero4 = {0.f, 0.f, 0.f, 0.f};
      f32x4 ar[2], ai[2], yr[2] = {zero4, zero4}, yi[2] = {zero4, zero4};
      const int npass = b == 0 ? 2 : 1;
      const unsigned wo = w4 * 2048 + l * 32;
#pragma unroll 1
      for (int pass = 0; pass < npass; ++pass) {
        const unsigned wcol = WS_WT + (b + pass * 256) * CS_;
        const u32x4 wv0 = __builtin_amdgcn_raw_buffer_load_b128(rs, wo, wcol, SC1);
        const u32x4 wv1 = __builtin_amdgcn_raw_buffer_load_b128(rs, wo + 16, wcol, SC1);
        float fre[8], fim[8];
#pragma unroll
        for (int e = 0; e < 8; ++e) { const unsigned v = e < 4 ? wv0[e] : wv1[e - 4]; fre[e] = bf_lo(v); fim[e] = bf_hi(v); }
        f16x8 z[2][4];
#pragma unroll
        for (int t = 0; t < 2; ++t)
#pragma unroll
          for (int s = 0; s < 4; ++s) z[t][s] = __builtin_bit_cast(f16x8, zr[t][s]);
        if (b == 0) {
          const unsigned mcol = WS_WT + (257 + pass) * CS_;
          const u32x4 mv0 = __builtin_amdgcn_raw_buffer_load_b128(rs, wo, mcol, SC1);
          const u32x4 mv1 = __builtin_amdgcn_raw_buffer_load_b128(rs, wo + 16, mcol, SC1);
#pragma unroll
          for (int e = 0; e < 8; ++e) {
            const unsigned v = e < 4 ? mv0[e] : mv1[e - 4];
            const float re = 0.5f * fre[e] + 0.5f * bf_lo(v), im = 0.5f * fim[e] - 0.5f * bf_hi(v);
            fre[e] = pass ? -im : re; fim[e] = pass ? re : im;
          }
#pragma unroll
          for (int t = 0; t < 2; ++t)
#pragma unroll
            for (int s = 0; s < 4; ++s) z[t][s] = __builtin_bit_cast(f16x8, (zr[t][s] >> (pass * 16)) & 0xffffu);
        }
#pragma unroll
        for (int t = 0; t < 2; ++t) {
          ar[t] = zero4; ai[t] = zero4;
#pragma unroll
          for (int s = 0; s < 4; ++s) { ar[t] = MFMA16H(z[t][s], c64[0][s], ar[t]); ai[t] = MFMA16H(z[t][s], c64[1][s], ai[t]); }
        }
#pragma unroll
        for (int t = 0; t < 2; ++t)
#pragma unroll
          for (int r = 0; r < 4; ++r) {
            const float c = twc[4 * t + r], s = tws[4 * t + r], xr = ar[t][r], xi = ai[t][r];
            ar[t][r] = xr * c + xi * s; ai[t][r] = xi * c - xr * s;
          }
        const f16x8 fr = pack8h(ar[0], ar[1]), fi = pack8h(ai[0], ai[1]), fn = negh(fr);
#pragma unroll
        for (int u = 0; u < 2; ++u) {
          ar[u] = MFMA16H(c32[0][u], fr, zero4); ar[u] = MFMA16H(c32[1][u], fi, ar[u]);
          ai[u] = MFMA16H(c32[0][u], fi, zero4); ai[u] = MFMA16H(c32[1][u], fn, ai[u]);
        }
#pragma unroll
        for (int u = 0; u < 2; ++u)
#pragma unroll
          for (int r = 0; r < 4; ++r) {
            const float c = fre[4 * u + r], s = fim[4 * u + r], xr = ar[u][r] * 0x1p-16f, xi = ai[u][r] * 0x1p-16f;
            yr[u][r] += xr * c - xi * s; yi[u][r] += xi * c + xr * s;
          }
      }
      STAMP(15);
      float mxf = 0.f;
#pragma unroll
      for (int u = 0; u < 2; ++u)
#pragma unroll
        for (int r = 0; r < 4; ++r) mxf = fmaxf(mxf, fmaxf(fabsf(yr[u][r]), fabsf(yi[u][r])));
      const unsigned ex = min(max(max64u(fbits(mxf)) >> 23, 12u), 250u);
      const float wsc = __builtin_bit_cast(float, (262u - ex) << 23);
      if (l == 0) *(LAS unsigned*)(L_MX + w4 * 4) = ex;
      const f16x8 fr = pack8h(yr[0] * wsc, yr[1] * wsc), fi = pack8h(yi[0] * wsc, yi[1] * wsc), fn = negh(fi);
#pragma unroll
      for (int t = 0; t < 2; ++t) {
        ar[t] = MFMA16H(c32[0][t], fr, zero4); ar[t] = MFMA16H(c32[1][t], fn, ar[t]);
        ai[t] = MFMA16H(c32[0][t], fi, zero4); ai[t] = MFMA16H(c32[1][t], fr, ai[t]);
      }
#pragma unroll
      for (int t = 0; t < 2; ++t)
#pragma unroll
        for (int r = 0; r < 4; ++r) {
          const float c = twc[4 * t + r], s = tws[4 * t + r], xr = ar[t][r], xi = ai[t][r];
          *(LAS unsigned*)(L_CREG + t * 4096 + w4 * 1024 + (((l >> 2) & 3) * 16 + 4 * q + r) * 16 + (l & 3) * 4) = pk_h(xr * c - xi * s, -(xi * c + xr * s));
        }
      sync4(C_EX, 4, l);
      const u32x4 m4 = *(const LAS u32x4*)(L_MX);
      f16x8 z[2][4];
#pragma unroll
      for (int t = 0; t < 2; ++t)
#pragma unroll
        for (int s = 0; s < 4; ++s) z[t][s] = *(const LAS f16x8*)(L_CREG + t * 4096 + s * 1024 + l * 16);
      const unsigned exm = max(max(m4[0], m4[1]), max(m4[2], m4[3]));
#pragma unroll
      for (int s = 0; s < 4; ++s) {
        const unsigned d = min(exm - m4[s], 40u);
        const _Float16 f = (_Float16)__builtin_bit_cast(float, (127u - d) << 23);
        z[0][s] *= f; z[1][s] *= f;
      }
      const float osc = __builtin_bit_cast(float, (exm - 8u) << 23), osc2 = b == 0 ? 256.f : 512.f;
#pragma unroll
      for (int t = 0; t < 2; ++t) {
        ar[t] = zero4; ai[t] = zero4;
#pragma unroll
        for (int s = 0; s < 4; ++s) { ar[t] = MFMA16H(z[t][s], c64[0][s], ar[t]); ai[t] = MFMA16H(z[t][s], c64[1][s], ai[t]); }
      }
      u32x4 ore, oim;
#pragma unroll
      for (int t = 0; t < 2; ++t)
#pragma unroll
        for (int i = 0; i < 2; ++i) {
          ore[2 * t + i] = pk_bf(ar[t][2 * i] * osc2 * osc, ar[t][2 * i + 1] * osc2 * osc);
          oim[2 * t + i] = pk_bf(ai[t][2 * i] * -osc2 * osc, ai[t][2 * i + 1] * -osc2 * osc);
        }
      const unsigned yo = WS_Y1 + ((16 * w4 + (l & 15)) * 32 + 8 * q) * 2;
      __builtin_amdgcn_raw_buffer_store_b128(ore, rs, opaque(yo + b * YP_), 0, SC1);
      __builtin_amdgcn_raw_buffer_store_b128(oim, rs, opaque(yo + (256 + b) * YP_), 0, SC1);
      STAMP(8);
      wait_vm<0>();
      if (lds_arrive(C_Y, l) == 3 && l == 0) __builtin_amdgcn_raw_buffer_store_b64(u32x2{tag_lo, tag_hi}, rs, WS_F2 + b * 8, 0, SC1);
      STAMP(9);
    }
    bf16x8 gf[8][2];
#pragma unroll
    for (int t = 0; t < 2; ++t) {
      const int d = t64 * 64 + t * 32 + (l & 31);
      const float st = (float)d * (1.f / 512.f);
#pragma unroll
      for (int i = 0; i < 8; ++i) {
        const int c0 = (w4 & 1) * 128 + 16 * ((i + t64) & 7) + 8 * h;
        const float a0 = (float)((c0 * d + (w4 >> 1) * 128) & 511) * (1.f / 512.f);
        float cv[8];
#pragma unroll
        for (int e = 0; e < 8; ++e) cv[e] = __builtin_amdgcn_cosf(a0 + (float)e * st);
        if (c0 == 0 && w4 == 2) cv[0] = (d & 1) ? -1.f : 1.f;
        const u32x4 o = {pk_bf(cv[0], cv[1]), pk_bf(cv[2], cv[3]), pk_bf(cv[4], cv[5]), pk_bf(cv[6], cv[7])};
        gf[i][t] = __builtin_bit_cast(bf16x8, o);
        asm volatile("" : "+v"(gf[i][t]));
      }
    }
    STAMP(19);
    if (w == 6) { poll4<4>(rs, WS_F2 + l * 32, tag_lo, tag_hi); lds_set(R_Y); }
    else lds_wait(R_Y, 1);
    STAMP(10);
    u32x4 yv[16];
    {
      const unsigned yo = WS_Y1 + (128 * w4 + (l >> 3)) * YP_ + r64 * 128 + (l & 7) * 16;
#pragma unroll
      for (int i = 0; i < 16; ++i) yv[i] = __builtin_amdgcn_raw_buffer_load_b128(rs, yo, ((i + 2 * t64) & 15) * 8 * YP_, SC1);
    }
    f32x16 acc[2][2];
#pragma unroll
    for (int a = 0; a < 2; ++a)
#pragma unroll
      for (int t = 0; t < 2; ++t)
#pragma unroll
        for (int r = 0; r < 16; ++r) acc[a][t][r] = 0.f;
    auto cu = [&](int i) { return (i % 3) == 2 ? L_CREG2 : L_CREG + (i % 3) * 8192; };
    unsigned tc[2];
#pragma unroll
    for (int t = 0; t < 2; ++t)
      tc[t] = w4 * 2048 + (8 * h + q4) * 128 + (((4 * t + 2 * blk + (pp >> 1)) ^ (((q4 >> 1) & 1) << 2)) << 4) + (pp & 1) * 8;
    const unsigned wr0 = w4 * 2048 + (l >> 3) * 128 + swz;
    bf16x8 yf[2][2];
    auto ywr = [&](int i) { *(LAS u32x4*)(wr0 + cu(i)) = yv[2 * i]; *(LAS u32x4*)(wr0 + cu(i) + 1024) = yv[2 * i + 1]; };
    auto yrd = [&](int i) { yf[i & 1][0] = lds_tr2(tc[0] + cu(i), tc[0] + cu(i) + 512); yf[i & 1][1] = lds_tr2(tc[1] + cu(i), tc[1] + cu(i) + 512); };
    ywr(0); yrd(0); ywr(1);
#pragma unroll
    for (int i = 0; i < 8; ++i) {
      if (i + 1 < 8) yrd(i + 1);
      if (i + 2 < 8) ywr(i + 2);
      SCHED_FENCE();
      acc[0][0] = MFMA32(gf[i][0], yf[i & 1][0], acc[0][0]); acc[0][1] = MFMA32(gf[i][1], yf[i & 1][0], acc[0][1]);
      acc[1][0] = MFMA32(gf[i][0], yf[i & 1][1], acc[1][0]); acc[1][1] = MFMA32(gf[i][1], yf[i & 1][1], acc[1][1]);
      SCHED_FENCE();
    }
    STAMP(11);
    lds_wait(C_O, 4);
#pragma unroll
    for (int tl = 0; tl < 4; ++tl)
#pragma unroll
      for (int g = 0; g < 4; ++g) {
        const f32x16& o = acc[tl >> 1][tl & 1];
        *(LAS f32x4*)(wd_slot(4) + tl * 16384 + w4 * 4096 + g * 1024 + l * 16) = f32x4{o[4 * g], o[4 * g + 1], o[4 * g + 2], o[4 * g + 3]};
      }
    wait_lgkm<0>(); bar();
    STAMP(12);
    if (b == 0 && tid == 256) __builtin_amdgcn_raw_buffer_store_b64(u32x2{tag_lo, tag_hi}, rs, WS_SYNC, 0, SC1);
  }
}

extern "C" void kernel_launch(void* const* d_in, const int* in_sizes, int n_in, void* d_out, int out_size,
                              void* d_ws, size_t ws_size, hipStream_t stream) {
  Params P;
  P.x = (const char*)d_in[0]; P.wr = (const char*)d_in[1]; P.wi = (const char*)d_in[2];
  P.wg = (const char*)d_in[4]; P.wu = (const char*)d_in[5]; P.wd = (const char*)d_in[6];
  P.out = (char*)d_out; P.ws = (char*)d_ws;
  (void)hipFuncSetAttribute((const void*)fused_kernel, hipFuncAttributeMaxDynamicSharedMemorySize, LDS_BYTES);
  fused_kernel<<<256, 512, LDS_BYTES, stream>>>(P);
}
```

```cpp
#include <hip/hip_runtime.h>
#include <stdint.h>
#pragma clang diagnostic ignored "-Wint-to-pointer-cast"

#define DI __device__ __forceinline__
#define LAS __attribute__((address_space(3)))
typedef unsigned short u16;
typedef __attribute__((ext_vector_type(8))) __bf16 bf16x8;
typedef __attribute__((ext_vector_type(4))) __bf16 bf16x4;
typedef __attribute__((ext_vector_type(2))) __bf16 bf16x2;
typedef __attribute__((ext_vector_type(8))) _Float16 f16x8;
typedef __attribute__((ext_vector_type(2))) _Float16 f16x2;
typedef __attribute__((ext_vector_type(2))) float f32x2;
typedef __attribute__((ext_vector_type(4))) float f32x4;
typedef __attribute__((ext_vector_type(16))) float f32x16;
typedef __attribute__((ext_vector_type(4))) unsigned u32x4;
typedef __attribute__((ext_vector_type(2))) unsigned u32x2;

namespace tb {
constexpr double PI = 3.14159265358979323846264338327950288;
constexpr double cos_small(double x) {
  double x2 = x * x, t = 1.0, s = 1.0;
  for (int i = 1; i <= 12; ++i) { t *= -x2 / ((2 * i - 1) * (2 * i)); s += t; }
  return s;
}
constexpr double sin_small(double x) {
  double x2 = x * x, t = x, s = x;
  for (int i = 1; i <= 12; ++i) { t *= -x2 / ((2 * i) * (2 * i + 1)); s += t; }
  return s;
}
constexpr double cos512(int i) {
  const int n = 512;
  i &= 511;
  if (i > n / 2) i = n - i;
  bool neg = false;
  if (i > n / 4) { i = n / 2 - i; neg = true; }
  double v = (i == n / 4) ? 0.0 : (i <= n / 8) ? cos_small(2 * PI * i / n) : sin_small(2 * PI * (n / 4 - i) / n);
  return neg ? -v : v;
}
constexpr u16 f2bf(double d) {
  float f = (float)d;
  unsigned u = __builtin_bit_cast(unsigned, f);
  u += 0x7fffu + ((u >> 16) & 1u);
  return (u16)(u >> 16);
}
struct Cos512 { u16 v[512]; };
constexpr Cos512 mk_cos_bf() { Cos512 c{}; for (int i = 0; i < 512; ++i) c.v[i] = f2bf(cos512(i)); return c; }
constexpr Cos512 CB = mk_cos_bf();
struct Chunk { u16 v[16384]; };
constexpr Chunk mkF(int ci) {
  Chunk o{};
  const int q = ci >> 2, which = (ci >> 1) & 1, d0 = (ci & 1) * 128;
  for (int d = 0; d < 128; ++d) {
    const int dd = d0 + d;
    int idx = (which ? 128 : 0) + q * dd;
    u16* p = o.v + d * 128;
    for (int m = 0; m < 128; ++m) { p[m] = CB.v[idx & 511]; idx += 2 * dd; }
    if (which && !q) p[0] = CB.v[(dd & 1) * 256];
  }
  return o;
}
template <int I> struct FC { static constexpr Chunk v = mkF(I); };
struct Tables { Chunk F[8]; };
}
#define F_(i) tb::FC<i>::v
#if defined(__HIP_DEVICE_COMPILE__)
static __device__ __attribute__((aligned(256))) const tb::Tables g_tab = {{F_(0), F_(1), F_(2), F_(3), F_(4), F_(5), F_(6), F_(7)}};
#else
static __device__ __attribute__((aligned(256))) const tb::Tables g_tab = {};
#endif

constexpr unsigned CS_ = 8448;
constexpr unsigned WP_ = 8832;
constexpr unsigned YP_ = 4224;
constexpr unsigned WS_SYNC = 0;
constexpr unsigned WS_F0 = 256;
constexpr unsigned WS_FW = WS_F0 + 2048;
constexpr unsigned WS_FR = WS_FW + 2048;
constexpr unsigned WS_RS = 8192;
constexpr unsigned WS_CS = WS_RS + 2048 * 4;
constexpr unsigned WS_F1 = WS_CS + 65536;
constexpr unsigned WS_F2 = WS_F1 + 32768;
constexpr unsigned WS_X1 = WS_F2 + 65536;
constexpr unsigned WS_WT = WS_X1 + 256 * CS_;
constexpr unsigned WS_Y1 = WS_WT + 260 * WP_;
constexpr unsigned WS_H = WS_Y1 + 512 * YP_;
constexpr unsigned WS_END = WS_H + 64 * 112 * 1024;

struct Params {
  const char* x; const char* wr; const char* wi; const char* wg; const char* wu; const char* wd;
  char* out; char* ws;
};
typedef unsigned long long u64;
typedef __amdgpu_buffer_rsrc_t rsrc_t;
constexpr int SC1 = 16;

DI void glds16(unsigned voff, const void* sbase, unsigned ldsdst) {
  asm volatile("s_mov_b32 m0, %2\n\ts_nop 0\n\tglobal_load_lds_dwordx4 %0, %1" ::"v"(voff), "s"(sbase), "s"(ldsdst) : "memory");
}
DI void gload16(u32x4& dst, unsigned voff, const void* sbase) {
  asm volatile("global_load_dwordx4 %0, %1, %2" : "=v"(dst) : "v"(voff), "s"(sbase) : "memory");
}
DI void gload16_sc1(u32x4& dst, unsigned voff, const void* sbase) {
  asm volatile("global_load_dwordx4 %0, %1, %2 sc1" : "=v"(dst) : "v"(voff), "s"(sbase) : "memory");
}
DI void gload8(u32x2& dst, unsigned voff, const void* sbase) {
  asm volatile("global_load_dwordx2 %0, %1, %2" : "=v"(dst) : "v"(voff), "s"(sbase) : "memory");
}
DI void gload8_sc1(u32x2& dst, unsigned voff, const void* sbase) {
  asm volatile("global_load_dwordx2 %0, %1, %2 sc1" : "=v"(dst) : "v"(voff), "s"(sbase) : "memory");
}
template <int N> DI void wait_vm() { asm volatile("s_waitcnt vmcnt(%0)" ::"i"(N) : "memory"); }
template <int N> DI void wait_vm2(u32x4& a, u32x4& b) { asm volatile("s_waitcnt vmcnt(%2)" : "+v"(a), "+v"(b) : "i"(N) : "memory"); }
template <int N> DI void wait_lgkm() { asm volatile("s_waitcnt lgkmcnt(%0)" ::"i"(N) : "memory"); }
DI void bar() { asm volatile("s_barrier" ::: "memory"); }
#define SCHED_FENCE() __builtin_amdgcn_sched_barrier(0)
DI bf16x8 lds_rd128(unsigned a) { return *(const LAS bf16x8*)a; }
DI bf16x8 lds_tr2(unsigned a0, unsigned a1) {
  bf16x4 lo = __builtin_amdgcn_ds_read_tr16_b64_v4bf16((LAS bf16x4*)a0);
  bf16x4 hi = __builtin_amdgcn_ds_read_tr16_b64_v4bf16((LAS bf16x4*)a1);
  return __builtin_shufflevector(lo, hi, 0, 1, 2, 3, 4, 5, 6, 7);
}
DI float bfr(float v) { return (float)(__bf16)v; }
DI unsigned pk_bf(float a, float b) { f32x2 v = {a, b}; return __builtin_bit_cast(unsigned, __builtin_convertvector(v, bf16x2)); }
DI unsigned pk_h(float a, float b) { f32x2 v = {a, b}; return __builtin_bit_cast(unsigned, __builtin_convertvector(v, f16x2)); }
DI float bf_lo(unsigned u) { return __builtin_bit_cast(float, u << 16); }
DI float bf_hi(unsigned u) { return __builtin_bit_cast(float, u & 0xffff0000u); }
DI unsigned fbits(float f) { return __builtin_bit_cast(unsigned, f); }
DI float bitsf(unsigned u) { return __builtin_bit_cast(float, u); }
template <int CTRL, int RM> DI float dpp0(float v) {
  return __builtin_bit_cast(float, __builtin_amdgcn_update_dpp(0, __builtin_bit_cast(int, v), CTRL, RM, 0xf, RM == 0xf));
}
DI float sum8(float v) { v += dpp0<0x111, 0xf>(v); v += dpp0<0x112, 0xf>(v); v += dpp0<0x114, 0xf>(v); return v; }
DI float sum32(float v) { v = sum8(v); v += dpp0<0x118, 0xf>(v); v += dpp0<0x142, 0xa>(v); return v; }
DI float sum64u(float v) { v = sum32(v); v += dpp0<0x143, 0xc>(v); return __builtin_bit_cast(float, __builtin_amdgcn_readlane(__builtin_bit_cast(int, v), 63)); }
template <int CTRL, int RM> DI unsigned dppu(unsigned v) { return (unsigned)__builtin_amdgcn_update_dpp(0, (int)v, CTRL, RM, 0xf, RM == 0xf); }
DI unsigned max64u(unsigned v) {
  v = max(v, dppu<0x111, 0xf>(v)); v = max(v, dppu<0x112, 0xf>(v)); v = max(v, dppu<0x114, 0xf>(v)); v = max(v, dppu<0x118, 0xf>(v));
  v = max(v, dppu<0x142, 0xa>(v)); v = max(v, dppu<0x143, 0xc>(v));
  return (unsigned)__builtin_amdgcn_readlane((int)v, 63);
}
#define MFMA32(a, b, c) __builtin_amdgcn_mfma_f32_32x32x16_bf16((a), (b), (c), 0, 0, 0)
#define MFMA16H(a, b, c) __builtin_amdgcn_mfma_f32_16x16x32_f16((a), (b), (c), 0, 0, 0)
DI f16x8 pack8h(const f32x4& a, const f32x4& b) {
  u32x4 o = {pk_h(a[0], a[1]), pk_h(a[2], a[3]), pk_h(b[0], b[1]), pk_h(b[2], b[3])};
  return __builtin_bit_cast(f16x8, o);
}
DI f16x8 negh(const f16x8& a) { u32x4 o = __builtin_bit_cast(u32x4, a); o ^= 0x80008000u; return __builtin_bit_cast(f16x8, o); }

constexpr unsigned SPIN_CAP = 1u << 15;
constexpr int PRIO_MAT = 2, POLL_S = 10, POLL_F2 = 12, F2_DELAY = 26, CB_X = 32, CH_N = 1, G_DEPTH = 7, G_EARLY = 12;
constexpr unsigned WMAP = 0x32107654;
constexpr unsigned CODE_PF = 43000;
constexpr unsigned L_STAGE = 49152;
constexpr unsigned wd_slot(int k) { return k == 0 ? 98304u : k == 1 ? 114688u : k == 2 ? 131072u : k == 3 ? 0u : k == 4 ? 16384u : k == 5 ? 32768u : k == 6 ? 49152u : 65536u; }
constexpr unsigned PG_ = 1024 + 32, PS_ = 4 * PG_ + 128, L_SP = 0, L_GP = 16 * PS_;
constexpr unsigned L_CREG = 81920;
constexpr unsigned L_CREG2 = 147456;
constexpr unsigned L_MISC = 155648;
constexpr unsigned L_CTR = L_MISC, L_MX = L_MISC + 96, L_TAG = L_MISC + 128, L_PV = L_MISC + 256, L_ST = L_MISC + 1024, L_CSF = L_MISC + 2048;
constexpr unsigned C_H = L_CTR, C_Y = L_CTR + 4, C_EX = L_CTR + 8, C_O = L_CTR + 16, C_WD = L_CTR + 28, R_Y = L_CTR + 44;
constexpr unsigned LDS_BYTES = 160 * 1024;

DI unsigned opaque(unsigned v) { asm volatile("" : "+v"(v)); return v; }
DI void glds4(const unsigned (&vo)[4], const void* sbase, unsigned ldsdst) {
  asm volatile("s_mov_b32 m0, %5\n\ts_nop 0\n\tglobal_load_lds_dwordx4 %0, %4\n\tglobal_load_lds_dwordx4 %1, %4 offset:1024\n\t"
               "global_load_lds_dwordx4 %2, %4 offset:2048\n\tglobal_load_lds_dwordx4 %3, %4 offset:3072"
               ::"v"(vo[0]), "v"(vo[1]), "v"(vo[2]), "v"(vo[3]), "s"(sbase), "s"(ldsdst) : "memory");
}
DI void glds2(unsigned v0, unsigned v1, const void* sbase, unsigned ldsdst) {
  asm volatile("s_mov_b32 m0, %3\n\ts_nop 0\n\tglobal_load_lds_dwordx4 %0, %2\n\tglobal_load_lds_dwordx4 %1, %2 offset:1024"
               ::"v"(v0), "v"(v1), "s"(sbase), "s"(ldsdst) : "memory");
}
template <int CTRL> DI float dppm(float v) { return __builtin_bit_cast(float, __builtin_amdgcn_mov_dpp(__builtin_bit_cast(int, v), CTRL, 0xf, 0xf, true)); }
DI float allsum8(float v) { v += dppm<0xB1>(v); v += dppm<0x4E>(v); v += dppm<0x141>(v); return v; }
DI float add1(float a, float b) { float r; asm volatile("v_add_f32 %0, %1, %2" : "=v"(r) : "v"(a), "v"(b)); return r; }
DI float fma1(float a, float b, float c) { float r; asm volatile("v_fma_f32 %0, %1, %2, %3" : "=v"(r) : "v"(a), "v"(b), "v"(c)); return r; }
DI float fms1(float a, float b, float c) { float r; asm volatile("v_fma_f32 %0, %1, %2, -%3" : "=v"(r) : "v"(a), "v"(b), "v"(c)); return r; }
DI float relu(float v) { return __builtin_bit_cast(float, max(__builtin_bit_cast(int, v), 0)); }
DI unsigned lds_ld(unsigned a) { return __builtin_amdgcn_readfirstlane(__hip_atomic_load((LAS unsigned*)a, __ATOMIC_RELAXED, __HIP_MEMORY_SCOPE_WORKGROUP)); }
DI unsigned lds_arrive(unsigned ctr, int l) {
  unsigned old = 0;
  if (l == 0) old = __hip_atomic_fetch_add((LAS unsigned*)ctr, 1u, __ATOMIC_RELAXED, __HIP_MEMORY_SCOPE_WORKGROUP);
  return old;
}
template <int SL = 1> DI void lds_wait(unsigned ctr, unsigned target) {
#pragma unroll 1
  for (unsigned s = 0; s < (SPIN_CAP << 4); ++s) { if (lds_ld(ctr) >= target) break; __builtin_amdgcn_s_sleep(SL); }
  asm volatile("" ::: "memory");
}
DI void lds_set(unsigned a) { if ((threadIdx.x & 63) == 0) *(volatile LAS unsigned*)a = 1u; }
template <int SLEEP> DI void poll1(rsrc_t rs, unsigned off, unsigned lo, unsigned hi) {
  bool ok = false;
#pragma unroll 1
  for (unsigned s = 0; s < SPIN_CAP; ++s) {
    if (!ok) { const u32x2 f = __builtin_amdgcn_raw_buffer_load_b64(rs, off, 0, SC1); ok = f[0] == lo && f[1] == hi; }
    if (__all(ok)) break;
    __builtin_amdgcn_s_sleep(SLEEP);
  }
  asm volatile("" ::: "memory");
}
template <int SLEEP> DI void poll2(rsrc_t rs, unsigned off, unsigned off2, unsigned lo, unsigned hi) {
  bool ok = false;
#pragma unroll 1
  for (unsigned s = 0; s < SPIN_CAP; ++s) {
    if (!ok) {
      const u32x2 f = __builtin_amdgcn_raw_buffer_load_b64(rs, off, 0, SC1), g = __builtin_amdgcn_raw_buffer_load_b64(rs, off2, 0, SC1);
      ok = ((f[0] ^ lo) | (f[1] ^ hi) | (g[0] ^ lo) | (g[1] ^ hi)) == 0;
    }
    if (__all(ok)) break;
    __builtin_amdgcn_s_sleep(SLEEP);
  }
  asm volatile("" ::: "memory");
}
template <int SLEEP> DI void poll4(rsrc_t rs, unsigned off, unsigned lo, unsigned hi) {
  bool ok = false;
#pragma unroll 1
  for (unsigned s = 0; s < SPIN_CAP; ++s) {
    if (!ok) {
      const u32x4 f = __builtin_amdgcn_raw_buffer_load_b128(rs, off, 0, SC1), g = __builtin_amdgcn_raw_buffer_load_b128(rs, off + 1024, 0, SC1);
      ok = ((f[0] ^ lo) | (f[1] ^ hi) | (f[2] ^ lo) | (f[3] ^ hi) | (g[0] ^ lo) | (g[1] ^ hi) | (g[2] ^ lo) | (g[3] ^ hi)) == 0;
    }
    if (__all(ok)) break;
    __builtin_amdgcn_s_sleep(SLEEP);
  }
  asm volatile("" ::: "memory");
}
DI void sync4(unsigned ctr, unsigned target, int l) {
  wait_lgkm<0>();
  lds_arrive(ctr, l);
  lds_wait(ctr, target);
}
DI void gload_u16(unsigned& dst, unsigned voff, const void* sbase) {
  asm volatile("global_load_ushort %0, %1, %2" : "=v"(dst) : "v"(voff), "s"(sbase) : "memory");
}
constexpr int GW = 16, GH = G_DEPTH, GE = G_EARLY;
constexpr int g2_allow(int i) {
  int seq_w[44] = {}, seq_h[28] = {}, n = 0;
  for (int k = 0; k < GH; ++k) { n += 2; seq_h[k] = n; }
  for (int s = 0; s < i; ++s) {
    if ((s & 1) && s >= GE && s + GW < 28) { n += 4; seq_w[s - 1 + GW] = n; seq_w[s + GW] = n; }
    if (s + GH < 28) { n += 2; seq_h[s + GH] = n; }
  }
  int need = seq_h[i];
  if (i + 1 < 28 && seq_w[i + 1] > need) need = seq_w[i + 1];
  return n - need;
}

DI void tile_of(int b, int& p, int& j) {
  const int xcd = b & 7, idx = b >> 3, gn = (xcd >> 1) * 28 + idx - 4;
  p = (xcd & 1) * 8 + (idx < 4 ? (xcd >> 1) * 2 + (idx >> 1) : (gn & 7));
  j = idx < 4 ? 14 + (idx & 1) : (gn >> 3);
}

__global__ __launch_bounds__(512, 2) void fused_kernel(Params P) {
  const char* code0;
  asm volatile("s_getpc_b64 %0 ; code0" : "=s"(code0));
  const int l = threadIdx.x & 63, b = blockIdx.x;
  const int w = (WMAP >> (4 * __builtin_amdgcn_readfirstlane(threadIdx.x >> 6))) & 7;
  const int tid = w * 64 + l;
  const int h = l >> 5, w4 = w & 3;
  const rsrc_t rs = __builtin_amdgcn_make_buffer_rsrc((void*)P.ws, 0, (int)WS_END, 0x00020000);
  const int xcd = b & 7, idx = b >> 3;
  int p, j;
  tile_of(b, p, j);
  const bool dft = j >= 14;
  const char* tabF = (const char*)&g_tab.F[0] + (j & 1) * 131072;
  {
    const unsigned ldt = dft ? 256u : 3584u;
    const char* t1 = (dft ? tabF : P.wg + j * 256) + (size_t)(4 * w) * ldt;
    const char* t2 = (dft ? tabF + 65536 : P.wu + j * 256) + (size_t)(4 * w) * ldt;
    const unsigned wo = (l >> 4) * ldt + (((l & 15) ^ ((l >> 4) << 2)) << 4);
    const unsigned d0 = (dft ? 65536u : 0u) + w * 1024, d1 = d0 + (dft ? 16384u : 32768u);
    glds16(wo, t1, d0); glds16(wo ^ 32, t2, d0 + 8192);
    if (dft) { glds16(wo, t1 + 64 * ldt, d0 + 32768); glds16(wo ^ 32, t2 + 64 * ldt, d0 + 32768 + 8192); }
    else {
      const unsigned xo = (l >> 3) * 1024 + (((l & 7) ^ (l >> 4)) << 4);
      glds2(xo, (xo ^ 64) + 8192 - 1024, P.x + (size_t)(p * 128 + w * 16) * 1024, 16384 + w * 2048);
    }
    glds16(wo, t1 + 32 * ldt, d1); glds16(wo ^ 32, t2 + 32 * ldt, d1 + 8192);
  }
  const int r64 = xcd * 4 + (idx & 3), t64 = idx >> 2;
  const unsigned swz = ((l & 7) ^ (((l >> 4) & 1) << 2)) << 4;
  const unsigned wdo = (l >> 3) * 1024 + swz;
  const int blk = (l >> 4) & 1, q4 = (l & 15) >> 2, pp = l & 3;
  const char* wds = P.wd + (size_t)(448 * w4) * 1024 + t64 * 128;
  const unsigned rot2 = (unsigned)((7 * t64 + 14 * (r64 & 3)) >> 2) % 14u;
  const unsigned nblk = dft ? 4u : 8u;
  unsigned tag_lo, tag_hi;
  auto make_tag = [&](u32x2 ep) {
    u64 e = ((u64)(unsigned)__builtin_amdgcn_readfirstlane(ep[1]) << 32) | (unsigned)__builtin_amdgcn_readfirstlane(ep[0]);
    e += 0x9E3779B97F4A7C15ull;
    e = (e ^ (e >> 30)) * 0xBF58476D1CE4E5B9ull;
    e = (e ^ (e >> 27)) * 0x94D049BB133111EBull;
    e ^= e >> 31;
    tag_lo = (unsigned)e; tag_hi = (unsigned)(e >> 32);
  };
  u32x4 xq[8];
  if (dft) {
    const char* xs = P.x + (size_t)(p * 128 + w * 16 + (l >> 3)) * 1024 + (l & 7) * 16;
    u32x4 xr[8];
#pragma unroll
    for (int k = 0; k < 8; ++k) xq[k] = *(const u32x4*)(xs + (j & 1) * 8192 + k * 128);
#pragma unroll
    for (int k = 0; k < 8; ++k) xr[k] = *(const u32x4*)(xs + ((j & 1) ^ 1) * 8192 + k * 128);
    SCHED_FENCE();
    const float sg1 = (j & 1) ? -1.f : 1.f;
#pragma unroll
    for (int g = 0; g < 2; ++g) {
      const u32x4(&r)[8] = g ? xr : xq;
      const unsigned sr = w * 16 + (g ^ (j & 1)) * 8 + (l >> 3);
      const float pq = dppm<0x00>(bf_lo(r[0][0])), pm = dppm<0x141>(pq), pv = (l & 4) ? pm : pq;
      if ((l & 7) == 0) *(LAS u16*)(L_PV + sr * 2) = (u16)(fbits(pv) >> 16);
      const float pc = -(1.f + sg1) * pv;
#pragma unroll
      for (int v = 0; v < 4; ++v) {
        u32x4 o;
#pragma unroll
        for (int k = 0; k < 4; ++k) {
          const f32x2 u = (f32x2{bf_lo(r[v][k]), bf_hi(r[v][k])} + pc) + sg1 * f32x2{bf_lo(r[4 + v][k]), bf_hi(r[4 + v][k])};
          o[k] = pk_bf(u[0], u[1]);
        }
        *(LAS u32x4*)(v * 16384 + sr * 128 + (((l & 7) ^ ((sr >> 1) & 7)) << 4)) = o;
      }
    }
    if (tid < 128) *(LAS u32x4*)(L_CSF + tid * 16) = u32x4{(j == 14 && tid == 0) ? 0x44004400u : 0u, 0u, 0u, 0u};
    wait_lgkm<0>();
  }
  const unsigned ooff = (r64 * 64 + w * 8 + (l >> 3)) * 1024 + t64 * 128 + (l & 7) * 16;
  auto finish = [&](const u32x4& xv) {
    const unsigned tl = (w >> 2) * 2 + ((l >> 2) & 1), fo = (l & 3) * PG_ + ((w & 3) * 8 + (l >> 3)) * 16;
    u32x4 res;
#pragma unroll
    for (int hh = 0; hh < 2; ++hh) {
      const unsigned ss = L_SP + tl * (4 * PS_) + fo + hh * 512;
      const f32x4 s = (*(const LAS f32x4*)ss + *(const LAS f32x4*)(ss + PS_)) + (*(const LAS f32x4*)(ss + 2 * PS_) + *(const LAS f32x4*)(ss + 3 * PS_));
      const u32x2 gp = *(const LAS u32x2*)(L_GP + (tl * PS_ + fo + hh * 512) / 2);
      const f32x4 g = {bf_lo(gp[0]), bf_hi(gp[0]), bf_lo(gp[1]), bf_hi(gp[1])};
      float r[4];
#pragma unroll
      for (int e = 0; e < 4; ++e) {
        const float sg = bfr(bfr(s[e]) * g[e]);
        r[e] = ((e & 1) ? bf_hi(xv[2 * hh + (e >> 1)]) : bf_lo(xv[2 * hh + (e >> 1)])) + sg;
      }
      res[2 * hh] = pk_bf(r[0], r[1]); res[2 * hh + 1] = pk_bf(r[2], r[3]);
    }
    *(u32x4*)(P.out + ooff) = res;
  };

  if (w < 4) {
    {
      if (tid < 4) *(LAS u32x4*)(L_CTR + tid * 16) = u32x4{0u, 0u, 0u, 0u};
      unsigned cpf;
      gload_u16(cpf, min((unsigned)((l & 3) * 128 + w4 * 32 + idx) * 128u, (unsigned)CODE_PF), code0);
      unsigned xa[4], wa[2];
      const unsigned xrow = dft ? 8 * q4 + 4 * blk + pp : (l & 31);
#pragma unroll
      for (int k4 = 0; k4 < 4; ++k4) xa[k4] = xrow * 128 + (((2 * k4 + h) ^ ((xrow >> 1) & 7)) << 4);
#pragma unroll
      for (int tt = 0; tt < 2; ++tt)
        wa[tt] = blk * 8192 + (8 * h + q4) * 256 + (((4 * w4 + 2 * tt + (pp >> 1)) ^ ((q4 << 2) | (blk << 1))) << 4) + (pp & 1) * 8;
      f32x16 acc[4][2];
#pragma unroll
      for (int a = 0; a < 4; ++a)
#pragma unroll
        for (int t = 0; t < 2; ++t)
#pragma unroll
          for (int r = 0; r < 16; ++r) acc[a][t][r] = 0.f;
      bf16x8 xfA[4], wfA[2], xfB[4], wfB[2];
      auto load_frags = [&](unsigned wo, unsigned xo, int k4, bf16x8(&xfr)[4], bf16x8(&wfr)[2]) {
#pragma unroll
        for (int tt = 0; tt < 2; ++tt) wfr[tt] = lds_tr2(wa[tt] + wo + (k4 & 1) * 4096, wa[tt] + wo + (k4 & 1) * 4096 + 1024);
#pragma unroll
        for (int a = 0; a < 4; ++a) xfr[a] = lds_rd128(xa[k4] + xo + a * 4096);
      };
      auto compute = [&](bf16x8(&xfr)[4], bf16x8(&wfr)[2]) {
#pragma unroll
        for (int a = 0; a < 4; ++a)
#pragma unroll
          for (int tt = 0; tt < 2; ++tt) acc[a][tt] = MFMA32(wfr[tt], xfr[a], acc[a][tt]);
      };
      auto interleave = [&]() {
#pragma unroll
        for (int i = 0; i < 4; ++i) {
          __builtin_amdgcn_sched_group_barrier(0x008, 1, 0);
          __builtin_amdgcn_sched_group_barrier(0x002, 2, 0);
          __builtin_amdgcn_sched_group_barrier(0x080, 2, 0);
        }
        __builtin_amdgcn_sched_group_barrier(0x008, 4, 0);
        SCHED_FENCE();
      };
      asm volatile("" : "+v"(acc[0][0]), "+v"(acc[0][1]), "+v"(acc[1][0]), "+v"(acc[1][1]), "+v"(acc[2][0]), "+v"(acc[2][1]), "+v"(acc[3][0]), "+v"(acc[3][1]));
      asm volatile("" : "+v"(xa[0]), "+v"(xa[1]), "+v"(xa[2]), "+v"(xa[3]), "+v"(wa[0]), "+v"(wa[1]));
      __builtin_amdgcn_s_setprio(PRIO_MAT);
      wait_vm<1>();
      wait_lgkm<0>(); bar();
      unsigned slo = dft ? 0x55864u : 0x630630u, shi = dft ? 0x00475u : 0x852852u, sxx = dft ? 0x33210u : 0x741741u;
      load_frags((slo & 15) << 14, (sxx & 15) << 14, 0, xfA, wfA);
#pragma unroll 1
      for (unsigned v = 0; v < nblk; ++v) {
        const unsigned wlo = (slo & 15) << 14, whi = (shi & 15) << 14, xo = (sxx & 15) << 14;
        slo = (slo >> 4) | ((slo & 15) << 20); shi = (shi >> 4) | ((shi & 15) << 20); sxx = (sxx >> 4) | ((sxx & 15) << 20);
        load_frags(wlo, xo, 1, xfB, wfB);
        compute(xfA, wfA);
        interleave();
        load_frags(whi, xo, 2, xfA, wfA);
        compute(xfB, wfB);
        interleave();
        load_frags(whi, xo, 3, xfB, wfB);
        compute(xfA, wfA);
        interleave();
        if (v + 1 != nblk) { wait_lgkm<0>(); bar(); }
        load_frags((slo & 15) << 14, (sxx & 15) << 14, 0, xfA, wfA);
        compute(xfB, wfB);
        interleave();
      }
      float rstd[4];
      {
        bf16x8 fa[2], fb[4];
        u32x2 st[4], cc[2];
#pragma unroll
        for (int tt = 0; tt < 2; ++tt) cc[tt] = *(const LAS u32x2*)(L_CSF + (w4 * 64 + tt * 32 + (l & 31)) * 8);
#pragma unroll
        for (int a = 0; a < 4; ++a) st[a] = *(const LAS u32x2*)(L_ST + (a * 32 + xrow) * 8);
        const u32x2 tg = *(const LAS u32x2*)L_TAG;
        SCHED_FENCE();
        tag_lo = __builtin_amdgcn_readfirstlane(tg[0]); tag_hi = __builtin_amdgcn_readfirstlane(tg[1]);
#pragma unroll
        for (int a = 0; a < 4; ++a) {
          const u32x4 vb = {st[a][0], st[a][0] & 0xffffu, 0u, 0u};
          fb[a] = __builtin_bit_cast(bf16x8, vb);
          rstd[a] = bitsf(st[a][1]);
        }
#pragma unroll
        for (int tt = 0; tt < 2; ++tt) {
          const u32x4 va = {h ? 0u : cc[tt][0], h ? 0u : cc[tt][1], 0u, 0u};
          fa[tt] = __builtin_bit_cast(bf16x8, va);
        }
        compute(fb, fa);
      }
      if (!dft) {
#pragma unroll
        for (int a = 0; a < 4; ++a) {
          const float r2 = rstd[a] * rstd[a];
#pragma unroll
          for (int tt = 0; tt < 2; ++tt) {
            u32x4 o;
#pragma unroll
            for (int i = 0; i < 4; ++i)
              o[i] = pk_bf(relu(acc[a][tt][2 * i]) * acc[a][tt][8 + 2 * i] * r2, relu(acc[a][tt][2 * i + 1]) * acc[a][tt][9 + 2 * i] * r2);
            const int rt = p * 4 + a, ks = j * 8 + 2 * w4 + tt;
            __builtin_amdgcn_raw_buffer_store_b128(o, rs, opaque(l * 16 + WS_H + (rt * 112 + ks) * 1024), 0, SC1);
          }
        }
      } else {
        const unsigned lo = WS_X1 + p * 512 + (l & 31) * 16 + ((j - 14) + 2 * (w4 * 32 + 4 * h)) * CS_;
#pragma unroll
        for (int tt = 0; tt < 2; ++tt)
#pragma unroll
          for (int r = 0; r < 8; ++r) {
            u32x4 o;
#pragma unroll
            for (int a = 0; a < 4; ++a) o[a] = pk_h(acc[a][tt][r] * rstd[a], acc[a][tt][r + 8] * rstd[a]);
            __builtin_amdgcn_raw_buffer_store_b128(o, rs, opaque(lo + 2 * (tt * 16 + (r & 3) + 8 * (r >> 2)) * CS_), 0, SC1);
          }
      }
      __builtin_amdgcn_s_setprio(0);
      wait_vm<0>();
      asm volatile("" : "+v"(cpf));
      if (__builtin_amdgcn_readfirstlane(lds_arrive(C_H, l)) == 3) {
        if (dft) {
          const unsigned fo = WS_X1 + ((j - 14) + 2 * l) * CS_ + 8192 + p * 8;
          __builtin_amdgcn_raw_buffer_store_b64(u32x2{tag_lo, tag_hi}, rs, fo, 0, SC1);
          __builtin_amdgcn_raw_buffer_store_b64(u32x2{tag_lo, tag_hi}, rs, fo + 128 * CS_, 0, SC1);
        } else if (l < 16) __builtin_amdgcn_raw_buffer_store_b64(u32x2{tag_lo, tag_hi}, rs, WS_F1 + ((p * 16 + l) * 16 + j) * 8, 0, SC1);
      }
    }
    u32x4 xv;
    gload16(xv, ooff, P.x);
    f32x16 acc[2][2];
#pragma unroll
    for (int a = 0; a < 2; ++a)
#pragma unroll
      for (int t = 0; t < 2; ++t)
#pragma unroll
        for (int r = 0; r < 16; ++r) acc[a][t][r] = 0.f;
    auto kstep = [&](int i) { const unsigned k = 2 * rot2 + i; return k >= 28 ? k - 28 : k; };
    const char* hs0 = P.ws + WS_H + (size_t)((2 * r64 + (w4 >> 1)) * 112 + 28 * w4) * 1024;
    const char* hs1 = P.ws + WS_H + (size_t)((2 * r64 + ((w4 >> 1) ^ 1)) * 112 + 28 * w4) * 1024;
    const unsigned lo16 = l * 16;
    u32x4 rf[GH][2];
    auto issue_h = [&](int i) {
      gload16_sc1(rf[i % GH][0], lo16, hs0 + kstep(i) * 1024);
      gload16_sc1(rf[i % GH][1], lo16, hs1 + kstep(i) * 1024);
    };
    unsigned ta[2];
#pragma unroll
    for (int t = 0; t < 2; ++t)
      ta[t] = w4 * 4096 + (4 * h + q4) * 128 + (((4 * (t ^ (w4 & 1)) + 2 * blk + (pp >> 1)) ^ (((q4 >> 1) & 1) << 2)) << 4) + (pp & 1) * 8;
    bf16x8 ef[GE ? GE : 1][2], lf[2][2];
    unsigned dw[4];
#pragma unroll
    for (int i = 0; i < 4; ++i) dw[i] = wdo + i * (8192 - 1024);
    auto wd_pairs = [&](unsigned k0, unsigned k1) {
#pragma unroll 1
      for (unsigned k = k0; k < k1; ++k) {
        const unsigned sl = k & 7;
        glds4(dw, wds + kstep(2 * k) * 16384, (sl < 3 ? 98304u : 0u - 49152u) + sl * 16384 + w4 * 4096);
      }
    };
    lds_wait(C_H, 4);
    if (dft) { wd_pairs(0, 6); wait_vm<0>(); }
    else lds_wait(C_WD, 4);
#pragma unroll
    for (int i = 0; i < GE; ++i)
#pragma unroll
      for (int t = 0; t < 2; ++t) { const unsigned o = ta[t] + wd_slot(i >> 1) + (i & 1) * 2048; ef[i][t] = lds_tr2(o, o + 1024); }
#pragma unroll
    for (int i = 0; i < GE; ++i) asm volatile("" : "+v"(ef[i][0]), "+v"(ef[i][1]));
    wd_pairs(6, 8 + GE / 2);
    poll1<POLL_S>(rs, WS_F1 + ((r64 * 8 + t64) * 16 + (l < 14 ? l : 0)) * 8, tag_lo, tag_hi);
    lds_wait<2>(C_EX, 4);
#pragma unroll
    for (int i = 0; i < GH; ++i) issue_h(i);
    auto rd = [&](int i) {
      const unsigned o = wd_slot((i % GW) >> 1) + (i & 1) * 2048;
#pragma unroll
      for (int t = 0; t < 2; ++t) lf[i & 1][t] = lds_tr2(ta[t] + o, ta[t] + o + 1024);
    };
    if (GE == 0) rd(0);
#define G_STEP(i)                                                                                          \
  {                                                                                                        \
    wait_vm2<g2_allow(i)>(rf[(i) % GH][0], rf[(i) % GH][1]);                                                \
    if ((i) + 1 >= GE && (i) < 27) rd((i) + 1);                                                             \
    SCHED_FENCE();                                                                                          \
    const bf16x8 r0 = __builtin_bit_cast(bf16x8, rf[(i) % GH][0]), r1 = __builtin_bit_cast(bf16x8, rf[(i) % GH][1]); \
    const bf16x8 w0 = (i) < GE ? ef[(i) < GE ? (i) : 0][0] : lf[(i) & 1][0], w1 = (i) < GE ? ef[(i) < GE ? (i) : 0][1] : lf[(i) & 1][1]; \
    acc[0][0] = MFMA32(w0, r0, acc[0][0]); acc[0][1] = MFMA32(w1, r0, acc[0][1]);                            \
    acc[1][0] = MFMA32(w0, r1, acc[1][0]); acc[1][1] = MFMA32(w1, r1, acc[1][1]);                            \
    SCHED_FENCE();                                                                                          \
    if (((i) & 1) && (i) >= GE && (i) + GW < 28) glds4(dw, wds + kstep((i) - 1 + GW) * 16384, wd_slot(((i) % GW) >> 1) + w4 * 4096); \
    if ((i) + GH < 28) issue_h((i) + GH);                                                                   \
  }
    G_STEP(0) G_STEP(1) G_STEP(2) G_STEP(3) G_STEP(4) G_STEP(5) G_STEP(6) G_STEP(7) G_STEP(8) G_STEP(9)
    G_STEP(10) G_STEP(11) G_STEP(12) G_STEP(13) G_STEP(14) G_STEP(15) G_STEP(16) G_STEP(17) G_STEP(18)
    G_STEP(19) G_STEP(20) G_STEP(21) G_STEP(22) G_STEP(23) G_STEP(24) G_STEP(25) G_STEP(26) G_STEP(27)
#undef G_STEP
    asm volatile("" : "+v"(xv));
#pragma unroll
    for (int jj = 1; jj < 4; ++jj)
#pragma unroll
      for (int g = 0; g < 4; ++g) {
        const f32x16& o = acc[jj >> 1][jj & 1];
        *(LAS f32x4*)(wd_slot(jj - 1) + w4 * 4096 + g * 1024 + lo16) = f32x4{o[4 * g], o[4 * g + 1], o[4 * g + 2], o[4 * g + 3]};
      }
    sync4(C_O, 4, l);
#pragma unroll
    for (int jj = 1; jj < 4; ++jj)
#pragma unroll
      for (int g = 0; g < 4; ++g) {
        const f32x4 o = *(const LAS f32x4*)(wd_slot(jj - 1) + (w4 ^ jj) * 4096 + g * 1024 + lo16);
#pragma unroll
        for (int e = 0; e < 4; ++e) acc[0][0][4 * g + e] += o[e];
      }
#pragma unroll
    for (int r = 0; r < 16; ++r) {
      const float ob = bfr(acc[0][0][r]);
      acc[0][0][r] = ob * __builtin_amdgcn_rcpf(1.f + __expf(-ob));
    }
#pragma unroll
    for (int g = 0; g < 4; ++g)
      *(LAS u32x2*)(L_GP + (w4 * PS_ + g * PG_ + lo16) / 2) = u32x2{pk_bf(acc[0][0][4 * g], acc[0][0][4 * g + 1]), pk_bf(acc[0][0][4 * g + 2], acc[0][0][4 * g + 3])};
    wait_lgkm<0>(); bar();
    finish(xv);
  } else {
    const int hw = w & 1, cb = b ^ CB_X;
    unsigned dq[4], kd = rot2;
#pragma unroll
    for (int i = 0; i < 4; ++i) dq[i] = wdo + i * (8192 - 1024);
    const char* wdp = P.wd + (size_t)(896 * hw) * 1024 + t64 * 128;
    auto issue_d = [&](unsigned so) {
      const unsigned d = so + hw * 8192;
      glds4(dq, wdp + kd * 32768, d); glds4(dq, wdp + kd * 32768 + 448 * 1024, d + 4096);
      kd = kd == 13 ? 0 : kd + 1;
    };
    const int q = l >> 4;
    constexpr unsigned ld = 3584u;
    const char* mb1 = P.wg + j * 256;
    const char* mb2 = P.wu + j * 256;
    f32x4 twc[2], tws[2];
    f16x8 c64[2][4], c32[2][2];
    const int k1 = 16 * w4 + (l & 15);
    auto gen = [&]() {
#pragma unroll
      for (int jx = 0; jx < 8; ++jx) {
        const float a = (float)((8 * q + jx) * k1) * (1.f / 2048.f);
        twc[jx >> 2][jx & 3] = __builtin_amdgcn_cosf(a) * 0.125f; tws[jx >> 2][jx & 3] = __builtin_amdgcn_sinf(a) * 0.125f;
      }
      asm volatile("" : "+v"(twc[0]), "+v"(twc[1]), "+v"(tws[0]), "+v"(tws[1]));
#pragma unroll
      for (int s = 0; s < 4; ++s) {
        u32x4 o0, o1;
#pragma unroll
        for (int e = 0; e < 4; ++e) {
          const float a = (float)(k1 * (16 * s + 4 * q + e)) * (1.f / 64.f);
          const float c = __builtin_amdgcn_cosf(a), sn = __builtin_amdgcn_sinf(a);
          o0[e] = pk_h(c, sn); o1[e] = pk_h(-sn, c);
        }
        c64[0][s] = __builtin_bit_cast(f16x8, o0); c64[1][s] = __builtin_bit_cast(f16x8, o1);
        asm volatile("" : "+v"(c64[0][s]), "+v"(c64[1][s]));
      }
#pragma unroll
      for (int u = 0; u < 2; ++u) {
        const float a1 = (float)(8 * q4 + 4 * u + pp) * (1.f / 32.f);
        u32x4 o0, o1;
#pragma unroll
        for (int e = 0; e < 4; ++e) {
          const float a = a1 * (float)(8 * q + 2 * e), a2 = a + a1;
          o0[e] = pk_h(__builtin_amdgcn_cosf(a), __builtin_amdgcn_cosf(a2)); o1[e] = pk_h(__builtin_amdgcn_sinf(a), __builtin_amdgcn_sinf(a2));
        }
        c32[0][u] = __builtin_bit_cast(f16x8, o0); c32[1][u] = __builtin_bit_cast(f16x8, o1);
      }
      asm volatile("" : "+v"(c32[0][0]), "+v"(c32[0][1]), "+v"(c32[1][0]), "+v"(c32[1][1]));
    };
    if (w < 6 && dft) {
      unsigned wq[4];
#pragma unroll
      for (int i = 0; i < 4; ++i) wq[i] = ((l >> 4) * 256 + (((l & 15) ^ ((l >> 4) << 2)) << 4)) ^ (hw << 5);
      auto coef = [&](int u) {
        const unsigned d = (u < 5 ? 65536u + u * 16384 : u == 5 ? 65536u : u == 6 ? 81920u : 0u) + hw * 8192;
        glds4(wq, tabF + u * 8192 + hw * 65536, d); glds4(wq, tabF + u * 8192 + hw * 65536 + 4096, d + 4096);
      };
      bar();
      coef(3); coef(4);
      wait_vm<8>(); bar();
      coef(5); coef(6); coef(7);
      wait_vm<16>(); bar();
      {
        unsigned tch[4];
        const unsigned line = ((p * 2 + (j & 1)) * 2 + hw) * 224 + l;
#pragma unroll
        for (int i = 0; i < 4; ++i) gload_u16(tch[i], (line + (i < 3 ? i * 64 : 160)) * 128, P.wd);
        wait_vm<4>(); bar();
        asm volatile("s_waitcnt vmcnt(0)" : "+v"(tch[0]), "+v"(tch[1]), "+v"(tch[2]), "+v"(tch[3])::"memory");
      }
      tag_lo = lds_ld(L_TAG); tag_hi = lds_ld(L_TAG + 4);
    } else if (w < 6) {
      const char* xs = P.x + (size_t)(p * 128 + hw * 64) * 1024;
      const char* m1 = mb1 + (size_t)(16 * hw) * ld;
      const char* m2 = mb2 + (size_t)(16 * hw) * ld;
      unsigned xo[4], wo0[4], wo1[4];
#pragma unroll
      for (int i = 0; i < 4; ++i) {
        xo[i] = (l >> 3) * 1024 + (((l & 7) ^ ((i & 1) * 4 + (l >> 4))) << 4) + i * (8192 - 1024);
        wo0[i] = (l >> 4) * ld + (((l & 15) ^ ((l >> 4) << 2)) << 4) + i * (4 * ld - 1024);
        wo1[i] = wo0[i] ^ 32;
      }
      auto issue_x = [&](unsigned so) {
        const unsigned d = so + 16384 + hw * 8192;
        glds4(xo, xs, d); glds4(xo, xs + 32768, d + 4096);
        xs += 128;
      };
      auto issue_w = [&](unsigned so) {
        const unsigned d = so + hw * 4096;
        glds4(wo0, m1, d); glds4(wo1, m2, d + 8192);
        m1 += 32 * ld; m2 += 32 * ld;
      };
      xs += 128; m1 += 64 * ld; m2 += 64 * ld;
      gen();
      issue_w(L_STAGE); issue_x(L_STAGE);
      wait_vm<16>(); bar();
      issue_w(L_STAGE + 32768);
      issue_w(2 * L_STAGE); issue_x(2 * L_STAGE); issue_w(2 * L_STAGE + 32768);
      wait_vm<24>(); bar();
      unsigned so = 0;
#pragma unroll 1
      for (unsigned v = 3; v < nblk; ++v) {
        issue_w(so); issue_x(so); issue_w(so + 32768);
        wait_vm<24>(); bar();
        so = (so == 2 * L_STAGE) ? 0u : so + L_STAGE;
      }
      issue_d(wd_slot(0)); issue_d(wd_slot(1)); wait_vm<16>();
      bar();
#pragma unroll 1
      for (unsigned k = 2; k < 6; ++k) issue_d((k < 3 ? 98304u : 0u - 49152u) + k * 16384);
      tag_lo = lds_ld(L_TAG); tag_hi = lds_ld(L_TAG + 4);
    } else {
      const u32x2 ep = __builtin_amdgcn_raw_buffer_load_b64(rs, WS_SYNC, 0, SC1);
      if (w == 7) {
        if (!dft) {
          const char* xs = P.x + (size_t)(p * 128 + j * 8 + (l >> 3)) * 1024 + (l & 7) * 16;
#pragma unroll
          for (int k = 0; k < 8; ++k) xq[k] = *(const u32x4*)(xs + k * 128);
        }
        make_tag(ep);
        f32x2 sm = {0.f, 0.f}, sq = {0.f, 0.f};
#pragma unroll
        for (int k = 0; k < 32; ++k) sm += f32x2{bf_lo(xq[k >> 2][k & 3]), bf_hi(xq[k >> 2][k & 3])};
        const float mean = allsum8(sm[0] + sm[1]) * (1.f / 512.f);
#pragma unroll
        for (int k = 0; k < 32; ++k) { const f32x2 d = f32x2{bf_lo(xq[k >> 2][k & 3]), bf_hi(xq[k >> 2][k & 3])} - mean; sq += d * d; }
        const float var = allsum8(sq[0] + sq[1]) * (1.f / 512.f);
        const float sd = bfr(sqrtf(bfr(bfr(var) + 1e-5f)));
        if ((l & 7) == 0) __builtin_amdgcn_raw_buffer_store_b32((fbits(bfr(mean)) >> 16) | fbits(sd), rs, WS_RS + (p * 128 + j * 8 + (l >> 3)) * 4, 0, SC1);
        if (!dft) bar();
        wait_vm<0>();
        if (l == 0) __builtin_amdgcn_raw_buffer_store_b64(u32x2{tag_lo, tag_hi}, rs, WS_FR + (p * 16 + j) * 8, 0, SC1);
        if (dft) bar();
        bar();
        u32x4 wv[8]; unsigned vxr, vxi;
        const int xc = min(l >> 3, 2), rg = b >> 2;
        {
          const unsigned o = ((16 * (rg >> 4) + 4 * (rg & 3) + (l >> 4) + 512 * ((rg >> 2) & 3) + 256 * ((l >> 3) & 1)) * 257 + 64 * (b & 3) + 8 * (l & 7)) * 2;
#pragma unroll
          for (int e = 0; e < 4; ++e) gload16(wv[e], o, P.wr + e * 32896);
          bar();
#pragma unroll
          for (int e = 0; e < 4; ++e) gload16(wv[4 + e], o, P.wi + e * 32896);
          int k = 16 * (b >> 6) + (b & 15) + 64 * (8 * ((b >> 4) & 3) + (l & 7));
          if (xc) k = (2048 - k) & 2047;
          const unsigned o2 = (k * 257 + (xc == 1 ? 0 : 256)) * 2;
          gload_u16(vxr, o2, P.wr); gload_u16(vxi, o2, P.wi);
        }
        bar();
        if (!dft) bar();
        asm volatile("s_waitcnt vmcnt(0)" : "+v"(wv[0]), "+v"(wv[1]), "+v"(wv[2]), "+v"(wv[3]), "+v"(wv[4]), "+v"(wv[5]), "+v"(wv[6]), "+v"(wv[7]), "+v"(vxr), "+v"(vxi)::"memory");
        const int srcl = (((l & 7) << 3) | (l >> 3)) << 2;
#pragma unroll
        for (int e = 0; e < 8; ++e)
#pragma unroll
          for (int i = 0; i < 4; ++i) wv[e][i] = (unsigned)__builtin_amdgcn_ds_bpermute(srcl, (int)wv[e][i]);
        const unsigned d = WS_WT + (64 * (b & 3) + 8 * (l >> 3)) * WP_ + rg * 128 + (l & 7) * 16;
#pragma unroll
        for (int c = 0; c < 8; ++c) {
          u32x4 o;
#pragma unroll
          for (int e = 0; e < 4; ++e) o[e] = __builtin_amdgcn_perm(wv[4 + e][c >> 1], wv[e][c >> 1], (c & 1) ? 0x07060302u : 0x05040100u);
          __builtin_amdgcn_raw_buffer_store_b128(o, rs, opaque(d + c * WP_), 0, SC1);
          if (c == 3 && !dft) bar();
        }
        if (l < 24) __builtin_amdgcn_raw_buffer_store_b32(vxr | (vxi << 16), rs, WS_WT + (256 + xc) * WP_ + b * 32 + (l & 7) * 4, 0, SC1);
        if (!dft) bar();
        wait_vm<0>();
        __builtin_amdgcn_raw_buffer_store_b64(u32x2{tag_lo, tag_hi}, rs, WS_WT + (64 * (b & 3) + l) * WP_ + 8192 + rg * 8, 0, SC1);
        if (l == 0) __builtin_amdgcn_raw_buffer_store_b64(u32x2{tag_lo, tag_hi}, rs, WS_FW + b * 8, 0, SC1);
        if (!dft) bar();
      } else if (dft) {
        make_tag(ep);
        if (l == 0) *(LAS u32x2*)L_TAG = u32x2{tag_lo, tag_hi};
        bar();
        u32x2 f = __builtin_amdgcn_raw_buffer_load_b64(rs, WS_FR + (p * 16 + (l & 15)) * 8, 0, SC1);
        bar();
        asm volatile("" : "+v"(f));
        if (!__all(f[0] == tag_lo && f[1] == tag_hi)) poll1<1>(rs, WS_FR + (p * 16 + (l & 15)) * 8, tag_lo, tag_hi);
        asm volatile("" ::: "memory");
        u32x2 rsv = __builtin_amdgcn_raw_buffer_load_b64(rs, WS_RS + (p * 128 + 2 * l) * 4, 0, SC1);
        bar();
        asm volatile("" : "+v"(rsv));
        const unsigned nm0 = (rsv[0] & 0xffffu) ^ 0x8000u, nm1 = (rsv[1] & 0xffffu) ^ 0x8000u, pv2 = *(const LAS unsigned*)(L_PV + l * 4);
        *(LAS u32x4*)(L_ST + l * 16) = u32x4{nm0 | (pv2 << 16), fbits(__builtin_amdgcn_rcpf(bf_hi(rsv[0]))), nm1 | (pv2 & 0xffff0000u), fbits(__builtin_amdgcn_rcpf(bf_hi(rsv[1])))};
        wait_lgkm<0>();
        bar();
      } else {
        u32x4 sv[16];
        const char* csb = ((p & 2) ? mb2 : mb1) + (p & 1) * 128 + (size_t)((p >> 2) * 128 + (l >> 3)) * ld + (l & 7) * 16;
#pragma unroll
        for (int i = 0; i < 16; ++i) sv[i] = *(const u32x4*)(csb + (size_t)(8 * i) * ld);
        make_tag(ep);
        if (l == 0) *(LAS u32x2*)L_TAG = u32x2{tag_lo, tag_hi};
        wait_vm<16>(); bar();
        f32x2 cs[4] = {{0.f, 0.f}, {0.f, 0.f}, {0.f, 0.f}, {0.f, 0.f}};
#pragma unroll
        for (int k = 0; k < 16; ++k) {
          asm volatile("" : "+v"(sv[k]));
#pragma unroll
          for (int i = 0; i < 4; ++i) { cs[i][0] = add1(cs[i][0], bf_lo(sv[k][i])); cs[i][1] = add1(cs[i][1], bf_hi(sv[k][i])); }
          if (k == 6) bar();
        }
        float c4[4], c2[2];
#pragma unroll
        for (int i = 0; i < 4; ++i) {
          const auto r = __builtin_amdgcn_permlane32_swap(fbits(cs[i >> 1][i & 1]), fbits(cs[2 + (i >> 1)][i & 1]), false, false);
          c4[i] = bitsf(r[0]) + bitsf(r[1]);
        }
#pragma unroll
        for (int i = 0; i < 2; ++i) {
          const auto r = __builtin_amdgcn_permlane16_swap(fbits(c4[i]), fbits(c4[2 + i]), false, false);
          c2[i] = bitsf(r[0]) + bitsf(r[1]);
        }
        const float t0 = c2[0] + dppm<0x128>(c2[0]), t1 = c2[1] + dppm<0x128>(c2[1]);
        const int f = (p & 1) * 64 + (l & 7) * 8 + h * 4 + blk * 2 + ((l >> 3) & 1);
        const int ci = (f >> 4) * 32 + ((p >> 1) & 1) * 16 + (f & 15);
        __builtin_amdgcn_raw_buffer_store_b32(fbits((l & 8) ? t1 : t0), rs, WS_CS + ((j * 4 + (p >> 2)) * 256 + ci) * 4, 0, SC1);
        bar();
        wait_vm<0>();
        if (l == 0) __builtin_amdgcn_raw_buffer_store_b64(u32x2{tag_lo, tag_hi}, rs, WS_F0 + (j * 16 + p) * 8, 0, SC1);
        bar();
        __builtin_amdgcn_s_sleep(6);
        const unsigned fo = ((l & 16) ? WS_FR + p * 128 : WS_F0 + j * 128) + (l & 15) * 8;
        u32x2 fl = __builtin_amdgcn_raw_buffer_load_b64(rs, fo, 0, SC1);
        bar();
        asm volatile("" : "+v"(fl));
        if (!__all(fl[0] == tag_lo && fl[1] == tag_hi)) poll1<1>(rs, fo, tag_lo, tag_hi);
        asm volatile("" ::: "memory");
        u32x2 rsv = __builtin_amdgcn_raw_buffer_load_b64(rs, WS_RS + (p * 128 + 2 * l) * 4, 0, SC1);
        u32x4 cv[4];
#pragma unroll
        for (int k = 0; k < 4; ++k) cv[k] = __builtin_amdgcn_raw_buffer_load_b128(rs, WS_CS + (j * 4 + k) * 1024 + l * 16, 0, SC1);
        bar();
        asm volatile("" : "+v"(rsv), "+v"(cv[0]), "+v"(cv[1]), "+v"(cv[2]), "+v"(cv[3]));
        const unsigned nm0 = (rsv[0] & 0xffffu) ^ 0x8000u, nm1 = (rsv[1] & 0xffffu) ^ 0x8000u;
        *(LAS u32x4*)(L_ST + l * 16) = u32x4{nm0 | (nm0 << 16), fbits(__builtin_amdgcn_rcpf(bf_hi(rsv[0]))), nm1 | (nm1 << 16), fbits(__builtin_amdgcn_rcpf(bf_hi(rsv[1])))};
        u32x4 o[2];
#pragma unroll
        for (int i = 0; i < 4; ++i) {
          const float c = add1(add1(bitsf(cv[0][i]), bitsf(cv[1][i])), add1(bitsf(cv[2][i]), bitsf(cv[3][i])));
          const float c0 = bf_hi(fbits(c)), r1 = add1(c, -c0), c1 = bf_hi(fbits(r1)), c2_ = add1(r1, -c1);
          o[i >> 1][2 * (i & 1)] = (fbits(c0) >> 16) | fbits(c1); o[i >> 1][2 * (i & 1) + 1] = fbits(c2_) >> 16;
        }
        asm volatile("" : "+v"(o[0]), "+v"(o[1]));
        bar();
        *(LAS u32x4*)(L_CSF + l * 32) = o[0]; *(LAS u32x4*)(L_CSF + l * 32 + 16) = o[1];
        wait_lgkm<0>();
        bar();
      }
    }
    if (!dft) __builtin_amdgcn_s_setprio(3);
    bf16x8 gf[8][2];
    auto gen_gf = [&](int t) {
      const int d = t64 * 64 + t * 32 + (l & 31);
      const float st = (float)d * (1.f / 512.f);
      const int c0 = (w4 & 1) * 128 + 8 * h;
      const float a0 = (float)((c0 * d + (w4 >> 1) * 128) & 511) * (1.f / 512.f);
      const float k1_ = 2.f * __builtin_amdgcn_cosf(st), k16 = 2.f * __builtin_amdgcn_cosf(16.f * st);
      float p0 = __builtin_amdgcn_cosf(a0 - 16.f * st), p1 = __builtin_amdgcn_cosf(a0 - 15.f * st);
      float q0 = __builtin_amdgcn_cosf(a0), q1 = __builtin_amdgcn_cosf(a0 + st);
#pragma unroll
      for (int i = 0; i < 8; ++i) {
        float cv[8];
        cv[0] = q0; cv[1] = q1;
#pragma unroll
        for (int e = 2; e < 8; ++e) cv[e] = fms1(k1_, cv[e - 1], cv[e - 2]);
        if (i < 7) { const float n0 = fms1(k16, q0, p0), n1 = fms1(k16, q1, p1); p0 = q0; p1 = q1; q0 = n0; q1 = n1; }
        if (i == 0 && w4 == 2 && h == 0) cv[0] = (d & 1) ? -1.f : 1.f;
        const u32x4 o = {pk_bf(cv[0], cv[1]), pk_bf(cv[2], cv[3]), pk_bf(cv[4], cv[5]), pk_bf(cv[6], cv[7])};
        gf[i][t] = __builtin_bit_cast(bf16x8, o);
        asm volatile("" : "+v"(gf[i][t]));
      }
    };
    {
      if (dft || w >= 6) gen();
      wait_vm<0>(); lds_arrive(C_WD, l);
      poll1<POLL_S>(rs, cb == 0 ? WS_FW + (w4 * 64 + l) * 8 : WS_WT + cb * WP_ + 8192 + w4 * 128 + (l & 15) * 8, tag_lo, tag_hi);
      const unsigned wo = w4 * 2048 + l * 32;
      u32x4 wv0 = __builtin_amdgcn_raw_buffer_load_b128(rs, wo, WS_WT + cb * WP_, SC1);
      u32x4 wv1 = __builtin_amdgcn_raw_buffer_load_b128(rs, wo + 16, WS_WT + cb * WP_, SC1);
      u32x4 xw[6];
      if (cb == 0) {
#pragma unroll
        for (int i = 0; i < 6; ++i) xw[i] = __builtin_amdgcn_raw_buffer_load_b128(rs, wo + (i & 1) * 16, WS_WT + (256 + (i >> 1)) * WP_, SC1);
      }
      lds_wait<4>(C_H, dft ? 4 : CH_N);
      poll1<POLL_S>(rs, WS_X1 + cb * CS_ + 8192 + (l & 15) * 8, tag_lo, tag_hi);
      __builtin_amdgcn_s_setprio(3);
      u32x4 zr[2][4];
#pragma unroll
      for (int t = 0; t < 2; ++t)
#pragma unroll
        for (int s = 0; s < 4; ++s) zr[t][s] = __builtin_amdgcn_raw_buffer_load_b128(rs, q * 512 + (l & 15) * 16 + t * 256 + s * 2048, WS_X1 + cb * CS_, SC1);
      const f32x4 zero4 = {0.f, 0.f, 0.f, 0.f};
      f32x4 ar[2], ai[2], yr[2] = {zero4, zero4}, yi[2] = {zero4, zero4};
      const int npass = cb == 0 ? 2 : 1;
#pragma unroll 1
      for (int pass = 0; pass < npass; ++pass) {
        if (pass) { wv0 = xw[0]; wv1 = xw[1]; }
        f32x4 fre[2], fim[2];
#pragma unroll
        for (int e = 0; e < 8; ++e) { const unsigned v = e < 4 ? wv0[e] : wv1[e - 4]; fre[e >> 2][e & 3] = bf_lo(v); fim[e >> 2][e & 3] = bf_hi(v); }
        f16x8 z[2][4];
        {
          const u32x4 mv0 = cb ? wv0 ^ 0x80000000u : pass ? xw[4] : xw[2], mv1 = cb ? wv1 ^ 0x80000000u : pass ? xw[5] : xw[3];
#pragma unroll
          for (int e = 0; e < 8; ++e) {
            const unsigned v = e < 4 ? mv0[e] : mv1[e - 4];
            const float re = 0.5f * fre[e >> 2][e & 3] + 0.5f * bf_lo(v), im = 0.5f * fim[e >> 2][e & 3] - 0.5f * bf_hi(v);
            fre[e >> 2][e & 3] = pass ? -im : re; fim[e >> 2][e & 3] = pass ? re : im;
          }
#pragma unroll
          for (int t = 0; t < 2; ++t)
#pragma unroll
            for (int s = 0; s < 4; ++s) z[t][s] = __builtin_bit_cast(f16x8, (zr[t][s] >> (pass * 16)) & (cb ? 0xffffffffu : 0xffffu));
        }
#pragma unroll
        for (int t = 0; t < 2; ++t) {
          ar[t] = zero4; ai[t] = zero4;
#pragma unroll
          for (int s = 0; s < 4; ++s) { ar[t] = MFMA16H(z[t][s], c64[0][s], ar[t]); ai[t] = MFMA16H(z[t][s], c64[1][s], ai[t]); }
        }
#pragma unroll
        for (int t = 0; t < 2; ++t) { const f32x4 xr = ar[t], xi = ai[t]; ar[t] = xr * twc[t] + xi * tws[t]; ai[t] = xi * twc[t] - xr * tws[t]; }
        const f16x8 fr = pack8h(ar[0], ar[1]), fi = pack8h(ai[0], ai[1]), fn = negh(fr);
#pragma unroll
        for (int u = 0; u < 2; ++u) {
          ar[u] = MFMA16H(c32[0][u], fr, zero4); ar[u] = MFMA16H(c32[1][u], fi, ar[u]);
          ai[u] = MFMA16H(c32[0][u], fi, zero4); ai[u] = MFMA16H(c32[1][u], fn, ai[u]);
        }
#pragma unroll
        for (int u = 0; u < 2; ++u) {
          const f32x4 xr = ar[u] * 0x1p-19f, xi = ai[u] * 0x1p-19f;
          yr[u] += xr * fre[u] - xi * fim[u]; yi[u] += xi * fre[u] + xr * fim[u];
        }
      }
      float mxf = 0.f;
#pragma unroll
      for (int u = 0; u < 2; ++u)
#pragma unroll
        for (int r = 0; r < 4; ++r) mxf = fmaxf(mxf, fmaxf(fabsf(yr[u][r]), fabsf(yi[u][r])));
      const unsigned ex = min(max(max64u(fbits(mxf)) >> 23, 12u), 250u);
      const float wsc = __builtin_bit_cast(float, (262u - ex) << 23);
      if (l == 0) *(LAS unsigned*)(L_MX + w4 * 4) = ex;
      const f16x8 fr = pack8h(yr[0] * wsc, yr[1] * wsc), fi = pack8h(yi[0] * wsc, yi[1] * wsc), fn = negh(fi);
#pragma unroll
      for (int t = 0; t < 2; ++t) {
        ar[t] = MFMA16H(c32[0][t], fr, zero4); ar[t] = MFMA16H(c32[1][t], fn, ar[t]);
        ai[t] = MFMA16H(c32[0][t], fi, zero4); ai[t] = MFMA16H(c32[1][t], fr, ai[t]);
      }
#pragma unroll
      for (int t = 0; t < 2; ++t) {
        const f32x4 br = ar[t] * twc[t] - ai[t] * tws[t], bi = -(ai[t] * twc[t]) - ar[t] * tws[t];
#pragma unroll
        for (int r = 0; r < 4; ++r)
          *(LAS unsigned*)(L_CREG2 + t * 4096 + w4 * 1024 + (((l >> 2) & 3) * 16 + 4 * q + r) * 16 + (l & 3) * 4) = pk_h(br[r], bi[r]);
      }
      sync4(C_EX, 4, l);
      const u32x4 m4 = *(const LAS u32x4*)(L_MX);
      f16x8 z[2][4];
#pragma unroll
      for (int t = 0; t < 2; ++t)
#pragma unroll
        for (int s = 0; s < 4; ++s) z[t][s] = *(const LAS f16x8*)(L_CREG2 + t * 4096 + s * 1024 + l * 16);
      const unsigned exm = max(max(m4[0], m4[1]), max(m4[2], m4[3]));
#pragma unroll
      for (int s = 0; s < 4; ++s) {
        const unsigned d = min(exm - m4[s], 40u);
        const _Float16 f = (_Float16)__builtin_bit_cast(float, (127u - d) << 23);
        z[0][s] *= f; z[1][s] *= f;
      }
      const float osc = __builtin_bit_cast(float, (exm - 8u) << 23), osc2 = cb == 0 ? 32.f : 64.f;
#pragma unroll
      for (int t = 0; t < 2; ++t) {
        ar[t] = zero4; ai[t] = zero4;
#pragma unroll
        for (int s = 0; s < 4; ++s) { ar[t] = MFMA16H(z[t][s], c64[0][s], ar[t]); ai[t] = MFMA16H(z[t][s], c64[1][s], ai[t]); }
      }
      u32x4 ore, oim;
#pragma unroll
      for (int t = 0; t < 2; ++t)
#pragma unroll
        for (int i = 0; i < 2; ++i) {
          ore[2 * t + i] = pk_bf(ar[t][2 * i] * osc2 * osc, ar[t][2 * i + 1] * osc2 * osc);
          oim[2 * t + i] = pk_bf(ai[t][2 * i] * -osc2 * osc, ai[t][2 * i + 1] * -osc2 * osc);
        }
      {
        const int srcl = (((l & 3) << 4) | (l >> 2)) << 2;
#pragma unroll
        for (int i = 0; i < 4; ++i) { ore[i] = (unsigned)__builtin_amdgcn_ds_bpermute(srcl, (int)ore[i]); oim[i] = (unsigned)__builtin_amdgcn_ds_bpermute(srcl, (int)oim[i]); }
      }
      const unsigned yo = WS_Y1 + w4 * 1024 + l * 16;
      __builtin_amdgcn_raw_buffer_store_b128(ore, rs, opaque(yo + cb * YP_), 0, SC1);
      __builtin_amdgcn_raw_buffer_store_b128(oim, rs, opaque(yo + (256 + cb) * YP_), 0, SC1);
      wait_vm<0>();
      if (__builtin_amdgcn_readfirstlane(lds_arrive(C_Y, l)) == 3 && l < 32) __builtin_amdgcn_raw_buffer_store_b64(u32x2{tag_lo, tag_hi}, rs, WS_F2 + l * 2048 + cb * 8, 0, SC1);
    }
    gen_gf(0);
    gen_gf(1);
    if (w == 6) { __builtin_amdgcn_s_sleep(F2_DELAY); poll4<POLL_F2>(rs, WS_F2 + r64 * 2048 + l * 16, tag_lo, tag_hi); lds_set(R_Y); }
    else lds_wait(R_Y, 1);
    const u32x4 xh = *(const u32x4*)(P.x + ooff);
    u32x4 yv[16];
    {
      const unsigned yo = WS_Y1 + (128 * w4 + (l >> 3)) * YP_ + r64 * 128 + (l & 7) * 16;
#pragma unroll
      for (int i = 0; i < 16; ++i) yv[i] = __builtin_amdgcn_raw_buffer_load_b128(rs, yo, i * 8 * YP_, SC1);
    }
    lds_wait(C_Y, 4); lds_wait(C_H, 4);
    f32x16 acc[2][2];
#pragma unroll
    for (int a = 0; a < 2; ++a)
#pragma unroll
      for (int t = 0; t < 2; ++t)
#pragma unroll
        for (int r = 0; r < 16; ++r) acc[a][t][r] = 0.f;
    auto cu = [&](int i) { return (i % 3) == 2 ? L_CREG2 : L_CREG + (i % 3) * 8192; };
    unsigned tc[2];
#pragma unroll
    for (int t = 0; t < 2; ++t)
      tc[t] = w4 * 2048 + (8 * h + q4) * 128 + (((4 * t + 2 * blk + (pp >> 1)) ^ (((q4 >> 1) & 1) << 2)) << 4) + (pp & 1) * 8;
    const unsigned wr0 = w4 * 2048 + (l >> 3) * 128 + swz;
    bf16x8 yf[2][2];
    auto ywr = [&](int i) { *(LAS u32x4*)(wr0 + cu(i)) = yv[2 * i]; *(LAS u32x4*)(wr0 + cu(i) + 1024) = yv[2 * i + 1]; };
    auto yrd = [&](int i) { yf[i & 1][0] = lds_tr2(tc[0] + cu(i), tc[0] + cu(i) + 512); yf[i & 1][1] = lds_tr2(tc[1] + cu(i), tc[1] + cu(i) + 512); };
    ywr(0); yrd(0); ywr(1);
#pragma unroll
    for (int i = 0; i < 8; ++i) {
      if (i + 1 < 8) yrd(i + 1);
      if (i + 2 < 8) ywr(i + 2);
      SCHED_FENCE();
      acc[0][0] = MFMA32(gf[i][0], yf[i & 1][0], acc[0][0]); acc[0][1] = MFMA32(gf[i][1], yf[i & 1][0], acc[0][1]);
      acc[1][0] = MFMA32(gf[i][0], yf[i & 1][1], acc[1][0]); acc[1][1] = MFMA32(gf[i][1], yf[i & 1][1], acc[1][1]);
      SCHED_FENCE();
    }
    lds_wait(C_O, 4);
#pragma unroll
    for (int tl = 0; tl < 4; ++tl)
#pragma unroll
      for (int g = 0; g < 4; ++g) {
        const f32x16& o = acc[tl >> 1][tl & 1];
        *(LAS f32x4*)(L_SP + tl * (4 * PS_) + w4 * PS_ + g * PG_ + l * 16) = f32x4{o[4 * g], o[4 * g + 1], o[4 * g + 2], o[4 * g + 3]};
      }
    wait_lgkm<0>(); bar();
    finish(xh);
    if (b == 0 && tid == 256) __builtin_amdgcn_raw_buffer_store_b64(u32x2{tag_lo, tag_hi}, rs, WS_SYNC, 0, SC1);
  }
}

extern "C" void kernel_launch(void* const* d_in, const int* in_sizes, int n_in, void* d_out, int out_size,
                              void* d_ws, size_t ws_size, hipStream_t stream) {
  Params P;
  P.x = (const char*)d_in[0]; P.wr = (const char*)d_in[1]; P.wi = (const char*)d_in[2];
  P.wg = (const char*)d_in[4]; P.wu = (const char*)d_in[5]; P.wd = (const char*)d_in[6];
  P.out = (char*)d_out; P.ws = (char*)d_ws;
  (void)hipFuncSetAttribute((const void*)fused_kernel, hipFuncAttributeMaxDynamicSharedMemorySize, LDS_BYTES);
  fused_kernel<<<256, 512, LDS_BYTES, stream>>>(P);
}
```
